```python
import torch

def _softmax(x, axis=-1):
    return torch.softmax(x, dim=axis)
def _gelu(x):
    return torch.nn.functional.gelu(x, approximate="tanh")
def _silu(x):
    return torch.nn.functional.silu(x)
def _sigmoid(x):
    return torch.sigmoid(x)
def _layernorm(x, axis=-1, eps=1e-5):
    m = x.mean(dim=axis, keepdim=True)
    v = x.var(dim=axis, keepdim=True, unbiased=False)
    return (x - m) / torch.sqrt(v + eps)
def _scatter_add(tgt, idx, src, axis=0):
    return tgt.clone().index_add_(axis, idx, src)
def _segment_sum(src, seg, n):
    out = torch.zeros((n,) + src.shape[1:], dtype=src.dtype)
    return out.index_add_(0, seg, src)
def _pad(x, pw):
    flat = tuple(v for pair in reversed(pw) for v in pair)
    return torch.nn.functional.pad(x, flat)

kernel_name = 'interleave_layers__mha_block__ssm_conv_h3__prompt'

def setup_inputs():

    _w = (lambda a: (lambda t: t.to(torch.bfloat16) if t.is_floating_point() else t)(torch.as_tensor(a)))
    def _A_setup():
        B, S, D, H = 1, 2048, 512, 32
        return {
            "x": _w((lambda *s: torch.randn(*s, dtype=torch.bfloat16) * 0.05)(B, S, D)),
            "wqkv": _w((lambda *s: torch.randn(*s, dtype=torch.bfloat16) * 0.05)(D, 3 * D)),
            "wo": _w((lambda *s: torch.randn(*s, dtype=torch.bfloat16) * 0.05)(D, D)),
        }
    _A_in = _A_setup()

    def _B_setup():
        B, S, D = 1, 2048, 512
        ks = 4
        return {
            "q": _w((lambda *s: torch.randn(*s, dtype=torch.bfloat16) * 0.05)(B, S, D)),
            "k": _w((lambda *s: torch.randn(*s, dtype=torch.bfloat16) * 0.4)(B, S, D)),
            "v": _w((lambda *s: torch.randn(*s, dtype=torch.bfloat16) * 0.4)(B, S, D)),
            "shift_k": _w((lambda *s: torch.randn(*s, dtype=torch.bfloat16) * 0.4)(D, ks) * 0.5),
            "ssm_k": _w((lambda *s: torch.randn(*s, dtype=torch.bfloat16) * 0.4)(D, S) * (1.0 / S ** 0.5)),
            "ssm_D": _w((lambda *s: torch.randn(*s, dtype=torch.bfloat16) * 0.4)(D)),
        }
    _B_in = _B_setup()

    return {
        "A_x": _A_in["x"],
        "A_wqkv": _A_in["wqkv"],
        "A_wo": _A_in["wo"],
        "B_q": _B_in["q"],
        "B_k": _B_in["k"],
        "B_v": _B_in["v"],
        "B_shift_k": _B_in["shift_k"],
        "B_ssm_k": _B_in["ssm_k"],
        "B_ssm_D": _B_in["ssm_D"],
    }

def reference(A_x, A_wqkv, A_wo, B_q, B_k, B_v, B_shift_k, B_ssm_k, B_ssm_D):
    def _A_ref(x, wqkv, wo):
        B, S, D = x.shape
        H = 32
        Dh = D // H
        qkv = (lambda x, *d: x.permute(*d))((x @ wqkv).reshape(B, S, 3, H, Dh), 2, 0, 3, 1, 4)
        q, k, v = qkv[0], qkv[1], qkv[2]
        scores = torch.einsum("bhqd,bhkd->bhqk", q, k) * (Dh ** -0.5)
        p = _softmax(scores, axis=-1)
        o = torch.einsum("bhqk,bhkd->bhqd", p, v)
        return (lambda x, *d: x.permute(*d))(o, 0, 2, 1, 3).reshape(B, S, D) @ wo

    def _B_ref(q, k, v, shift_k, ssm_k, ssm_D):
        B, S, D = q.shape
        ks = shift_k.shape[-1]
        k_pad = _pad(k, ((0, 0), (ks - 1, 0), (0, 0)))
        k_shift = torch.zeros_like(k)
        for t in range(ks):
            k_shift = k_shift + k_pad[:, t:t + S, :] * shift_k[:, ks - 1 - t][None, None, :]
        kv = k_shift * v
        L = 2 * S
        kv_pad = _pad(kv, ((0, 0), (0, S), (0, 0)))
        kf_pad = _pad(ssm_k, ((0, 0), (0, S)))
        kv_f = (lambda x, axes=(-2,-1): torch.fft.rfft2(x.float(), dim=axes))(kv_pad[:, None, :, :], axes=(1, 2))
        kf_f = (lambda x, axes=(-2,-1): torch.fft.rfft2(x.float(), dim=axes))(kf_pad[:, None, :], axes=(1, 2))
        kf_f = (lambda x, *d: x.permute(*d))(kf_f, 1, 2, 0)
        y_f = kv_f * kf_f[None, :, :, :]
        y = (lambda x, s, axes=(-2,-1): torch.fft.irfft2(x, s=s, dim=axes).to(torch.bfloat16))(y_f, (1, L), axes=(1, 2))[:, 0, :S, :]
        y = y + kv * ssm_D[None, None, :]
        return q * y

    x = A_x
    x = x + _A_ref(_layernorm(x, axis=-1), A_wqkv, A_wo)
    x = x + _B_ref(_layernorm(x, axis=-1), B_k, B_v, B_shift_k, B_ssm_k, B_ssm_D)
    x = x + _A_ref(_layernorm(x, axis=-1), A_wqkv, A_wo)
    x = x + _B_ref(_layernorm(x, axis=-1), B_k, B_v, B_shift_k, B_ssm_k, B_ssm_D)
    return x
```

```cpp
#include <hip/hip_runtime.h>
#include <hip/hip_bf16.h>
#include <stdint.h>

typedef __attribute__((ext_vector_type(2))) __bf16 bf16x2;
typedef __attribute__((ext_vector_type(8))) __bf16 bf16x8;
typedef __attribute__((ext_vector_type(16))) __bf16 bf16x16;
typedef __attribute__((ext_vector_type(2))) float f32x2;
typedef __attribute__((ext_vector_type(4))) float f32x4;
typedef __attribute__((ext_vector_type(8))) float f32x8;
typedef __attribute__((ext_vector_type(16))) float f32x16;
typedef __attribute__((ext_vector_type(2))) unsigned u32x2;
typedef __attribute__((ext_vector_type(4))) unsigned u32x4;
typedef __attribute__((ext_vector_type(4))) short s16x4;
typedef unsigned short u16;
#define LAS __attribute__((address_space(3)))
#define GAS __attribute__((address_space(1)))
typedef LAS char* lptr;

#define USE_SPARSE 1

constexpr int S = 2048, D = 512, NH = 32;
constexpr int UROW = 2304;
constexpr size_t MB = 1024 * 1024;
constexpr size_t WS_QF = 0, WS_KVF = 2 * MB, WS_OF = 6 * MB, WS_X2F = 8 * MB, WS_UREV = 10 * MB, WS_YT = 13 * MB,
                 WS_WOP = 15 * MB, WS_GRAN = 16 * MB;
constexpr float QSCALE = 0.25f * 1.4426950408889634f;

#define MFMA16(a, b, c) __builtin_amdgcn_mfma_f32_16x16x32_bf16((a), (b), (c), 0, 0, 0)
#define MFMA32(a, b, c) __builtin_amdgcn_mfma_f32_32x32x16_bf16((a), (b), (c), 0, 0, 0)

__device__ __forceinline__ bf16x8 asb(u32x4 v) { return __builtin_bit_cast(bf16x8, v); }
__device__ __forceinline__ unsigned pk2(float lo, float hi) {
  f32x2 f = {lo, hi};
  return __builtin_bit_cast(unsigned, __builtin_convertvector(f, bf16x2));
}
__device__ __forceinline__ float blo(unsigned u) { return __uint_as_float(u << 16); }
__device__ __forceinline__ float bhi(unsigned u) { return __uint_as_float(u & 0xffff0000u); }
__device__ __forceinline__ float rbf(float f) { return blo(pk2(f, f) & 0xffffu); }
__device__ __forceinline__ float dot2(unsigned a, unsigned b, float c) {
  return __builtin_amdgcn_fdot2_f32_bf16(__builtin_bit_cast(bf16x2, a), __builtin_bit_cast(bf16x2, b), c, false);
}
__device__ __forceinline__ void glds16(const void* g, unsigned lds_base) {
  unsigned sv;
  asm volatile("s_mov_b32 %0, m0\n\ts_mov_b32 m0, %2\n\ts_nop 0\n\tglobal_load_lds_dwordx4 %1, off\n\ts_mov_b32 m0, %0"
               : "=&s"(sv)
               : "v"(g), "s"(lds_base)
               : "memory");
}
__device__ __forceinline__ u32x2 trread(lptr p) {
  return __builtin_bit_cast(u32x2, __builtin_amdgcn_ds_read_tr16_b64_v4i16((LAS s16x4*)p));
}
__device__ __forceinline__ u32x4 ldsld(lptr p) { return *(LAS u32x4*)p; }
__device__ __forceinline__ int uni(int x) { return __builtin_amdgcn_readfirstlane(x); }
#define WAIT_BAR(N) asm volatile("s_waitcnt vmcnt(" #N ") lgkmcnt(0)\n\ts_barrier" ::: "memory")

struct __attribute__((packed, aligned(2))) U16x8 { u32x4 v; };

constexpr int QKV_LDS = 49152 + 8 * 48 * 4;
template <bool FRAGX>
__device__ __forceinline__ void qkv_phase(const char* __restrict__ xsrc, const u16* __restrict__ wqkv, char* __restrict__ qf,
                                          char* __restrict__ kvf, int h, int panel, lptr lds) {
  const int tid = threadIdx.x, lane = tid & 63, wid = uni(tid >> 6);
  const int n16 = lane & 15, g = lane >> 4;
  const unsigned lds0 = (unsigned)(uintptr_t)lds;
#pragma unroll
  for (int i = 0; i < 6; i++) {
    const int idx = wid * 6 + i, part = idx >> 4, blk = idx & 15;
    const int pr = blk * 32 + (lane >> 1);
    const int k = pr ^ (((pr >> 3) & 1) << 2);
    glds16(wqkv + (size_t)k * 1536 + part * 512 + h * 16 + (lane & 1) * 8, (unsigned)uni(lds0 + idx * 1024));
  }
  const char* xb = xsrc + (size_t)(panel * 16 + wid * 2) * 16384 + (FRAGX ? lane * 16 : n16 * 1024 + g * 16);
  u32x4 xf[2][16];
#pragma unroll
  for (int kk = 0; kk < 16; kk++)
#pragma unroll
    for (int t = 0; t < 2; t++) xf[t][kk] = *(const u32x4*)(xb + t * 16384 + kk * (FRAGX ? 1024 : 64));
  WAIT_BAR(32);
  f32x4 aq[2] = {}, ak[2] = {}, av[2] = {}, cq = {}, ck = {}, cv = {};
  float s1[2] = {0.f, 0.f}, s2[2] = {0.f, 0.f};
  const unsigned ONE2 = 0x3f803f80u;
  const u32x4 ones = {ONE2, ONE2, ONE2, ONE2};
  const int q4 = n16 >> 2, p4 = n16 & 3;
  const lptr wb0 = lds + (8 * g + (q4 ^ ((g & 1) << 2))) * 32 + p4 * 8;
  const lptr wb1 = lds + (8 * g + ((4 + q4) ^ ((g & 1) << 2))) * 32 + p4 * 8;
#pragma unroll
  for (int kk = 0; kk < 16; kk++) {
    u32x4 wf[3];
#pragma unroll
    for (int p = 0; p < 3; p++) {
      u32x2 a = trread(wb0 + p * 16384 + kk * 1024), b = trread(wb1 + p * 16384 + kk * 1024);
      wf[p] = u32x4{a[0], a[1], b[0], b[1]};
    }
#pragma unroll
    for (int t = 0; t < 2; t++) {
      aq[t] = MFMA16(asb(wf[0]), asb(xf[t][kk]), aq[t]);
      ak[t] = MFMA16(asb(wf[1]), asb(xf[t][kk]), ak[t]);
      av[t] = MFMA16(asb(xf[t][kk]), asb(wf[2]), av[t]);
#pragma unroll
      for (int e = 0; e < 4; e++) {
        s1[t] = dot2(xf[t][kk][e], ONE2, s1[t]);
        s2[t] = dot2(xf[t][kk][e], xf[t][kk][e], s2[t]);
      }
    }
    if ((kk >> 1) == wid) {
      cq = MFMA16(asb(wf[0]), asb(ones), cq);
      ck = MFMA16(asb(wf[1]), asb(ones), ck);
      cv = MFMA16(asb(ones), asb(wf[2]), cv);
    }
  }
  LAS float* cs = (LAS float*)(lds + 49152);
  if (n16 == 0) {
    *(LAS f32x4*)(cs + wid * 48 + 4 * g) = cq;
    *(LAS f32x4*)(cs + wid * 48 + 16 + 4 * g) = ck;
  }
  if (g == 0) cs[wid * 48 + 32 + n16] = cv[0];
  float mean[2], rstd[2];
#pragma unroll
  for (int t = 0; t < 2; t++) {
    float a = s1[t], b = s2[t];
    a += __shfl_xor(a, 16); b += __shfl_xor(b, 16);
    a += __shfl_xor(a, 32); b += __shfl_xor(b, 32);
    mean[t] = a * (1.f / D);
    float var = fmaxf(b * (1.f / D) - mean[t] * mean[t], 0.f);
    rstd[t] = __builtin_amdgcn_rsqf(var + 1e-5f);
  }
  WAIT_BAR(0);
  cq = f32x4{}; ck = f32x4{};
  float csv = 0.f;
#pragma unroll
  for (int w = 0; w < 8; w++) {
    cq += *(LAS f32x4*)(cs + w * 48 + 4 * g);
    ck += *(LAS f32x4*)(cs + w * 48 + 16 + 4 * g);
    csv += cs[w * 48 + 32 + n16];
  }
  const int tile = panel * 8 + wid;
  char* qdst = qf + ((size_t)h * 64 + tile) * 1024;
  char* kdst = kvf + ((size_t)h * 8 + panel) * 16384 + wid * 1024;
  char* vdst = kdst + 8192;
#pragma unroll
  for (int t = 0; t < 2; t++) {
    const float rq = rstd[t] * QSCALE;
    f32x4 qv = (aq[t] - mean[t] * cq) * rq, kv = (ak[t] - mean[t] * ck) * rstd[t];
    const int off = ((g >> 1) * 32 + t * 16 + n16) * 16 + (g & 1) * 8;
    *(u32x2*)(qdst + off) = u32x2{pk2(qv[0], qv[1]), pk2(qv[2], qv[3])};
    *(u32x2*)(kdst + off) = u32x2{pk2(kv[0], kv[1]), pk2(kv[2], kv[3])};
    float vv[4];
#pragma unroll
    for (int r = 0; r < 4; r++) {
      float m = __shfl(mean[t], 4 * g + r), rs = __shfl(rstd[t], 4 * g + r);
      vv[r] = (av[t][r] - m * csv) * rs;
    }
    const int voff = t * 512 + (g >> 1) * 256 + n16 * 16 + (g & 1) * 8;
    *(u32x2*)(vdst + voff) = u32x2{pk2(vv[0], vv[1]), pk2(vv[2], vv[3])};
  }
}

__device__ __forceinline__ void prep_phase(int e, const u16* __restrict__ bk, const u16* __restrict__ bv,
                                           const u16* __restrict__ shk, const u16* __restrict__ wo, char* __restrict__ ws,
                                           lptr lds) {
  const int tid = threadIdx.x;
  const int t0 = (e >> 3) * 64, c0 = (e & 7) * 64;
  const int r = tid >> 3, ch = tid & 7, t = t0 + r;
  u32x4 kr[4];
#pragma unroll
  for (int j = 0; j < 4; j++) {
    const int tt = t - j;
    kr[j] = u32x4{0u, 0u, 0u, 0u};
    if (tt >= 0) kr[j] = *(const u32x4*)(bk + (size_t)tt * D + c0 + ch * 8);
  }
  const u32x4 vr = *(const u32x4*)(bv + (size_t)t * D + c0 + ch * 8);
  u32x4 sk[4];
#pragma unroll
  for (int j = 0; j < 4; j++) sk[j] = *(const u32x4*)(shk + (size_t)(c0 + ch * 8) * 4 + j * 8);
  if (tid < 128) {
    const int f = e * 2 + (tid >> 6), lane = tid & 63;
    const int fg = f >> 5, i = (f >> 4) & 1, kk = f & 15, n = lane & 15, g = lane >> 4;
    const int c = 32 * fg + 8 * (n >> 2) + 4 * i + (n & 3);
    const int kb = 32 * kk + 16 * (g >> 1) + 4 * (g & 1);
    unsigned w[8];
#pragma unroll
    for (int j = 0; j < 8; j++) w[j] = wo[(size_t)(kb + 8 * (j >> 2) + (j & 3)) * D + c];
    *(u32x4*)(ws + WS_WOP + (size_t)f * 1024 + lane * 16) =
        u32x4{w[0] | (w[1] << 16), w[2] | (w[3] << 16), w[4] | (w[5] << 16), w[6] | (w[7] << 16)};
  } else if (tid < 192) {
    *(u32x4*)(ws + WS_GRAN + (size_t)e * 1024 + (tid - 128) * 16) = u32x4{0u, 0u, 0u, 0u};
  } else if (tid < 256) {
    const int i = tid - 192;
    *(u32x4*)(ws + WS_UREV + ((size_t)(e * 2 + (i >> 5)) * UROW + 2048 + (i & 31) * 8) * 2) = u32x4{0u, 0u, 0u, 0u};
  }
  LAS u16* tl = (LAS u16*)lds;
#pragma unroll
  for (int c = 0; c < 8; c++) {
    const unsigned s01 = sk[c >> 1][(c & 1) * 2], s23 = sk[c >> 1][(c & 1) * 2 + 1];
    float kx[4];
#pragma unroll
    for (int j = 0; j < 4; j++) kx[j] = (c & 1) ? bhi(kr[j][c >> 1]) : blo(kr[j][c >> 1]);
    float a = rbf(kx[3] * bhi(s23));
    a = rbf(a + rbf(kx[2] * blo(s23)));
    a = rbf(a + rbf(kx[1] * bhi(s01)));
    a = rbf(a + rbf(kx[0] * blo(s01)));
    const float vx = (c & 1) ? bhi(vr[c >> 1]) : blo(vr[c >> 1]);
    tl[(ch * 8 + c) * 72 + 63 - r] = (u16)(pk2(a * vx, 0.f) & 0xffffu);
  }
  __syncthreads();
  const u32x4 o = *(LAS u32x4*)(tl + r * 72 + ch * 8);
  *(u32x4*)(ws + WS_UREV + ((size_t)(c0 + r) * UROW + (1984 - t0) + ch * 8) * 2) = o;
}

template <int HALF>
__device__ __forceinline__ void conv_wave(int c, const u16* __restrict__ ssmk, const u16* __restrict__ ssmd,
                                          const char* __restrict__ ws_c, char* __restrict__ ws) {
  const int lane = threadIdx.x & 63, n = lane & 15, g = lane >> 4;
  constexpr int NJ = HALF ? 3 : 5;
  constexpr int NW = HALF ? 7 : 8;
  const u16* hrow = ssmk + (size_t)c * S;
  const u16* urow = (const u16*)(ws_c + WS_UREV) + (size_t)c * UROW;
  const u16* ap = hrow + (n + 8 * g - 15);
  const u16* bp = urow + (2032 - 16 * n + 8 * g);
  f32x4 acc[NJ] = {};
  u32x4 a0;
  {
    unsigned w[8];
#pragma unroll
    for (int j = 0; j < 8; j++) {
      const int idx = n + 8 * g - 15 + j;
      const unsigned v = hrow[idx < 0 ? 0 : idx];
      w[j] = idx < 0 ? 0u : v;
    }
    a0 = u32x4{w[0] | (w[1] << 16), w[2] | (w[3] << 16), w[4] | (w[5] << 16), w[6] | (w[7] << 16)};
  }
#pragma unroll 1
  for (int r = 0; r < 8; r++) {
    u32x4 A[NW], B[NW];
    if (r > 0) a0 = ((const U16x8*)(ap + 32 * r))->v;
    A[0] = a0;
#pragma unroll
    for (int w = 1; w < NW; w++) A[w] = ((const U16x8*)(ap + 32 * r + 256 * w))->v;
#pragma unroll
    for (int p = 0; p < NW; p++) B[p] = *(const u32x4*)(bp + 32 * r - 256 * p);
#pragma unroll
    for (int jj = 0; jj < NJ; jj++) {
      const int jt = HALF ? 4 + jj : (jj == 4 ? 7 : jj);
#pragma unroll
      for (int w = 0; w <= jt; w++) acc[jj] = MFMA16(asb(A[w]), asb(B[jt - w]), acc[jj]);
    }
  }
  const float dc = __uint_as_float((unsigned)ssmd[c] << 16);
  u16* yrow = (u16*)(ws + WS_YT) + (size_t)c * S;
#pragma unroll
  for (int jj = 0; jj < NJ; jj++) {
    const int jt = HALF ? 4 + jj : (jj == 4 ? 7 : jj);
    const int t = 256 * jt + 16 * n + 4 * g;
    const u32x2 u = *(const u32x2*)(urow + (2044 - t));
    const float y0 = rbf(acc[jj][0]) + rbf(bhi(u[1]) * dc), y1 = rbf(acc[jj][1]) + rbf(blo(u[1]) * dc);
    const float y2 = rbf(acc[jj][2]) + rbf(bhi(u[0]) * dc), y3 = rbf(acc[jj][3]) + rbf(blo(u[0]) * dc);
    *(u32x2*)(yrow + t) = u32x2{pk2(y0, y1), pk2(y2, y3)};
  }
}

constexpr int ATT_ONES = 131072;
constexpr int ATT_LDS = 131072 + 8192;
__device__ __forceinline__ float halfmax(float m) {
  auto rr = __builtin_amdgcn_permlane32_swap(__float_as_uint(m), __float_as_uint(m), false, false);
  return fmaxf(__uint_as_float(rr[0]), __uint_as_float(rr[1]));
}
__device__ __forceinline__ float max16(const f32x16& s) {
  float a = fmaxf(fmaxf(s[0], s[1]), s[2]), b = fmaxf(fmaxf(s[3], s[4]), s[5]);
  a = fmaxf(fmaxf(a, s[6]), s[7]); b = fmaxf(fmaxf(b, s[8]), s[9]);
  a = fmaxf(fmaxf(a, s[10]), s[11]); b = fmaxf(fmaxf(b, s[12]), s[13]);
  a = fmaxf(fmaxf(a, s[14]), s[15]);
  return fmaxf(a, b);
}
__device__ __forceinline__ void attn_phase(const char* __restrict__ qf, const char* __restrict__ kvf, char* __restrict__ of,
                                           int h, int panel, lptr lds) {
  const int tid = threadIdx.x, lane = tid & 63, wid = uni(tid >> 6);
  const unsigned lds0 = (unsigned)(uintptr_t)lds;
  const char* src = kvf + (size_t)h * 131072 + wid * 2048 + lane * 16;
  const bf16x8 qv = asb(*(const u32x4*)(qf + ((size_t)h * 64 + panel * 8 + wid) * 1024 + lane * 16));
  asm volatile("" ::"v"(qv));
#pragma unroll
  for (int p = 0; p < 8; p++) {
    glds16(src + p * 16384, (unsigned)uni(lds0 + p * 16384 + wid * 2048));
    glds16(src + p * 16384 + 1024, (unsigned)uni(lds0 + p * 16384 + wid * 2048 + 1024));
  }
  const unsigned ONE2 = 0x3f803f80u;
  *(LAS u32x4*)(lds + ATT_ONES + tid * 16) = u32x4{ONE2, ONE2, ONE2, ONE2};
  const bool vrow = (lane & 16) == 0;
  const lptr kb0 = lds + lane * 16;
  const lptr vb0 = vrow ? lds + 8192 + (lane >> 5) * 256 + (lane & 15) * 16 : lds + ATT_ONES + (lane & 15) * 16;
  const int vstep = vrow ? 16384 : 0;
  WAIT_BAR(14);
  float m;
  {
    f32x16 s = MFMA32(asb(ldsld(kb0)), qv, f32x16{});
    m = halfmax(max16(s));
  }
  f32x16 o;
#pragma unroll 1
  for (int pass = 0; pass < 2; pass++) {
    f32x16 negm;
#pragma unroll
    for (int r = 0; r < 16; r++) negm[r] = -m;
    o = f32x16{};
    lptr kb = kb0, vb = vb0;
#pragma unroll 1
    for (int p = 0; p < 8; p++) {
      if (pass == 0) {
        if (p == 1) WAIT_BAR(10);
        if (p == 3) WAIT_BAR(0);
      }
#pragma unroll
      for (int t = 0; t < 8; t++) {
        f32x16 s = MFMA32(asb(ldsld(kb + t * 1024)), qv, negm);
        const u32x4 v0 = ldsld(vb + t * 1024), v1 = ldsld(vb + t * 1024 + 512);
        f32x8 p0, p1;
#pragma unroll
        for (int r = 0; r < 8; r++) { p0[r] = __builtin_amdgcn_exp2f(s[r]); p1[r] = __builtin_amdgcn_exp2f(s[8 + r]); }
        o = __builtin_amdgcn_smfmac_f32_32x32x32_bf16(asb(v0), __builtin_bit_cast(bf16x16, p0), o, 0xDDDD, 0, 0);
        o = __builtin_amdgcn_smfmac_f32_32x32x32_bf16(asb(v1), __builtin_bit_cast(bf16x16, p1), o, 0xDDDD, 0, 0);
      }
      kb += 16384;
      vb += vstep;
    }
    if (pass == 1) break;
    float mag = o[8];
#pragma unroll
    for (int r = 0; r < 8; r++) mag = fmaxf(mag, fabsf(o[r]));
    bool ok = (mag < 1e30f);
#pragma unroll
    for (int r = 0; r < 9; r++) ok = ok && (o[r] == o[r]);
    if (__all(ok)) break;
    float mm = m;
#pragma unroll 1
    for (int t = 0; t < 64; t++) {
      f32x16 s = MFMA32(asb(ldsld(kb0 + (t >> 3) * 16384 + (t & 7) * 1024)), qv, f32x16{});
      mm = fmaxf(mm, max16(s));
    }
    m = halfmax(mm);
  }
  const float il = 1.f / o[8];
  const u32x4 ov = {pk2(o[0] * il, o[1] * il), pk2(o[2] * il, o[3] * il), pk2(o[4] * il, o[5] * il), pk2(o[6] * il, o[7] * il)};
  const int tile16 = panel * 16 + wid * 2 + ((lane >> 4) & 1);
  *(u32x4*)(of + ((size_t)tile16 * 16 + (h >> 1)) * 1024 + (((h & 1) * 2 + (lane >> 5)) * 16 + (lane & 15)) * 16) = ov;
}

__device__ __forceinline__ void hp_of_block(int b, int& h, int& panel) {
  h = (b & 7) * 4 + ((b >> 3) & 3);
  panel = b >> 5;
}

__global__ void __launch_bounds__(512, 2) k_qkv_first(const u16* x, const u16* wqkv, const u16* wo, const u16* bk, const u16* bv,
                                                      const u16* shk, char* ws) {
  extern __shared__ __attribute__((aligned(16))) char smem[];
  const int b = blockIdx.x;
  if (b < 256) {
    int h, panel;
    hp_of_block(b, h, panel);
    qkv_phase<false>((const char*)x, wqkv, ws + WS_QF, ws + WS_KVF, h, panel, (lptr)smem);
  } else {
    prep_phase(b - 256, bk, bv, shk, wo, ws, (lptr)smem);
  }
}

__global__ void __launch_bounds__(512, 2) k_qkv_second(const u16* wqkv, char* ws) {
  extern __shared__ __attribute__((aligned(16))) char smem[];
  int h, panel;
  hp_of_block(blockIdx.x, h, panel);
  qkv_phase<true>(ws + WS_X2F, wqkv, ws + WS_QF, ws + WS_KVF, h, panel, (lptr)smem);
}

template <bool CONV>
__global__ void __launch_bounds__(CONV ? 768 : 512, CONV ? 3 : 2) k_attn(const u16* ssmk, const u16* ssmd, char* ws) {
  extern __shared__ __attribute__((aligned(16))) char smem[];
  int h, panel;
  hp_of_block(blockIdx.x, h, panel);
  const int wid = uni(threadIdx.x >> 6);
  if (CONV && wid >= 8) {
    __builtin_amdgcn_s_barrier();
    __builtin_amdgcn_s_barrier();
    __builtin_amdgcn_s_barrier();
    const int c = blockIdx.x * 2 + ((wid - 8) >> 1);
    if (wid & 1) conv_wave<1>(c, ssmk, ssmd, ws, ws);
    else conv_wave<0>(c, ssmk, ssmd, ws, ws);
    return;
  }
  attn_phase(ws + WS_QF, ws + WS_KVF, ws + WS_OF, h, panel, (lptr)smem);
}

constexpr int OP_Y = 32768, OP_ST = 32768 + 8192, OP_LDS = OP_ST + 4 * 64 * 4 + 64 * 4;
template <bool LAST>
__global__ void __launch_bounds__(256, 1) k_oproj(const u16* x, char* ws, u16* out) {
  extern __shared__ __attribute__((aligned(16))) char smem[];
  const lptr lds = (lptr)smem;
  const unsigned lds0 = (unsigned)(uintptr_t)lds;
  const int b = blockIdx.x, rg = (b & 7) * 8 + (b >> 5), fq = (b >> 3) & 3;
  const int tid = threadIdx.x, lane = tid & 63, wid = uni(tid >> 6), n16 = lane & 15, g = lane >> 4;
  const int fg = fq * 4 + wid;
  const char* osrc = ws + WS_OF + (size_t)rg * 32768 + wid * 8192 + lane * 16;
#pragma unroll
  for (int i = 0; i < 8; i++) glds16(osrc + i * 1024, (unsigned)uni(lds0 + wid * 8192 + i * 1024));
  const char* ysrc = ws + WS_YT + (size_t)(fg * 32 + (lane >> 2)) * (S * 2) + rg * 64 + (lane & 3) * 16;
  glds16(ysrc, (unsigned)uni(lds0 + OP_Y + wid * 2048));
  glds16(ysrc + 16 * S * 2, (unsigned)uni(lds0 + OP_Y + wid * 2048 + 1024));
  const char* wsrc = ws + WS_WOP + (size_t)fg * 32768 + lane * 16;
  u32x4 wf[2][16];
#pragma unroll
  for (int kk = 0; kk < 16; kk++)
#pragma unroll
    for (int i = 0; i < 2; i++) wf[i][kk] = *(const u32x4*)(wsrc + i * 16384 + kk * 1024);
  u32x4 xr[2];
#pragma unroll
  for (int t = 0; t < 2; t++) {
    if (LAST) xr[t] = *(const u32x4*)(ws + WS_X2F + ((size_t)(rg * 2 + t) * 16 + fg) * 1024 + lane * 16);
    else xr[t] = *(const u32x4*)(x + (size_t)(rg * 32 + t * 16 + n16) * D + fg * 32 + g * 8);
  }
  WAIT_BAR(34);
  f32x4 acc[2][2] = {};
#pragma unroll
  for (int kk = 0; kk < 16; kk++) {
#pragma unroll
    for (int t = 0; t < 2; t++) {
      const bf16x8 ofr = asb(ldsld(lds + t * 16384 + kk * 1024 + lane * 16));
#pragma unroll
      for (int i = 0; i < 2; i++) acc[t][i] = MFMA16(asb(wf[i][kk]), ofr, acc[t][i]);
    }
  }
  float x1[2][8];
  LAS float* st = (LAS float*)(lds + OP_ST);
#pragma unroll
  for (int t = 0; t < 2; t++) {
    float a = 0.f, q = 0.f;
#pragma unroll
    for (int j = 0; j < 8; j++) {
      const float xv = (j & 1) ? bhi(xr[t][j >> 1]) : blo(xr[t][j >> 1]);
      const float v = rbf(xv + rbf(acc[t][j >> 2][j & 3]));
      x1[t][j] = v;
      a += v;
      q += v * v;
    }
    a += __shfl_xor(a, 16); q += __shfl_xor(q, 16);
    a += __shfl_xor(a, 32); q += __shfl_xor(q, 32);
    if (g == 0) st[wid * 64 + t * 16 + n16] = a;
    if (g == 1) st[wid * 64 + 32 + t * 16 + n16] = q;
  }
  WAIT_BAR(0);
  if (wid == 0) {
    const float part = (st[lane] + st[64 + lane]) + (st[128 + lane] + st[192 + lane]);
    GAS unsigned long long* gr = (GAS unsigned long long*)(ws + WS_GRAN) + ((size_t)(LAST ? 64 : 0) + rg) * 256 + lane;
    __hip_atomic_store(gr + fq * 64, (1ull << 32) | __float_as_uint(part), __ATOMIC_RELAXED, __HIP_MEMORY_SCOPE_AGENT);
    float v[4];
    for (unsigned spins = 0; spins < (1u << 20); spins++) {
      bool ok = true;
#pragma unroll
      for (int k = 0; k < 4; k++) {
        const unsigned long long w = __hip_atomic_load(gr + k * 64, __ATOMIC_RELAXED, __HIP_MEMORY_SCOPE_AGENT);
        v[k] = __uint_as_float((unsigned)w);
        ok = ok && (w >> 32) == 1ull;
      }
      if (__all(ok)) break;
      __builtin_amdgcn_s_sleep(1);
    }
    st[256 + lane] = (v[0] + v[1]) + (v[2] + v[3]);
  }
  WAIT_BAR(0);
  const lptr yb = lds + OP_Y + wid * 2048 + (8 * g + (n16 >> 2)) * 64 + (n16 & 3) * 8;
#pragma unroll
  for (int t = 0; t < 2; t++) {
    const float mean = st[256 + t * 16 + n16] * (1.f / D);
    const float var = fmaxf(st[256 + 32 + t * 16 + n16] * (1.f / D) - mean * mean, 0.f);
    const float rstd = __builtin_amdgcn_rsqf(var + 1e-5f);
    const u32x2 ya = trread(yb + t * 32), yc = trread(yb + 256 + t * 32);
    const float yv[8] = {blo(ya[0]), bhi(ya[0]), blo(ya[1]), bhi(ya[1]), blo(yc[0]), bhi(yc[0]), blo(yc[1]), bhi(yc[1])};
    float r[8];
#pragma unroll
    for (int j = 0; j < 8; j++) r[j] = x1[t][j] + rbf(rbf((x1[t][j] - mean) * rstd) * yv[j]);
    const u32x4 ov = {pk2(r[0], r[1]), pk2(r[2], r[3]), pk2(r[4], r[5]), pk2(r[6], r[7])};
    if (LAST) *(u32x4*)(out + (size_t)(rg * 32 + t * 16 + n16) * D + fg * 32 + g * 8) = ov;
    else *(u32x4*)(ws + WS_X2F + ((size_t)(rg * 2 + t) * 16 + fg) * 1024 + lane * 16) = ov;
  }
}

extern "C" void kernel_launch(void* const* d_in, const int* in_sizes, int n_in, void* d_out, int out_size, void* d_ws,
                              size_t ws_size, hipStream_t stream) {
  const u16* x = (const u16*)d_in[0];
  const u16* wqkv = (const u16*)d_in[1];
  const u16* wo = (const u16*)d_in[2];
  const u16* bk = (const u16*)d_in[4];
  const u16* bv = (const u16*)d_in[5];
  const u16* shk = (const u16*)d_in[6];
  const u16* ssmk = (const u16*)d_in[7];
  const u16* ssmd = (const u16*)d_in[8];
  char* ws = (char*)d_ws;
  u16* out = (u16*)d_out;
  (void)hipFuncSetAttribute((const void*)k_attn<true>, hipFuncAttributeMaxDynamicSharedMemorySize, ATT_LDS);
  (void)hipFuncSetAttribute((const void*)k_attn<false>, hipFuncAttributeMaxDynamicSharedMemorySize, ATT_LDS);
  k_qkv_first<<<512, 512, QKV_LDS, stream>>>(x, wqkv, wo, bk, bv, shk, ws);
  k_attn<true><<<256, 768, ATT_LDS, stream>>>(ssmk, ssmd, ws);
  k_oproj<false><<<256, 256, OP_LDS, stream>>>(x, ws, out);
  k_qkv_second<<<256, 512, QKV_LDS, stream>>>(wqkv, ws);
  k_attn<false><<<256, 512, ATT_LDS, stream>>>(ssmk, ssmd, ws);
  k_oproj<true><<<256, 256, OP_LDS, stream>>>(x, ws, out);
}
```

```cpp
#include <hip/hip_runtime.h>
#include <hip/hip_bf16.h>
#include <stdint.h>

#if defined(__HIP_DEVICE_COMPILE__)
#pragma clang attribute push(__attribute__((target("no-packed-fp32-ops"))), apply_to = function)
#endif

typedef __attribute__((ext_vector_type(2))) __bf16 bf16x2;
typedef __attribute__((ext_vector_type(8))) __bf16 bf16x8;
typedef __attribute__((ext_vector_type(2))) float f32x2;
typedef __attribute__((ext_vector_type(4))) float f32x4;
typedef __attribute__((ext_vector_type(16))) float f32x16;
typedef __attribute__((ext_vector_type(2))) unsigned u32x2;
typedef __attribute__((ext_vector_type(4))) unsigned u32x4;
typedef __attribute__((ext_vector_type(4))) short s16x4;
typedef unsigned short u16;
typedef unsigned long long u64;
#define LAS __attribute__((address_space(3)))
#define GAS __attribute__((address_space(1)))
typedef __amdgpu_buffer_rsrc_t rsrc_t;

constexpr int S = 2048, D = 512;
constexpr int MB = 1024 * 1024;
constexpr int WS_QF = 0, WS_KVF = 2 * MB, WS_KV2 = 6 * MB, WS_OF = 10 * MB  , WS_X2F = 14 * MB, WS_UREV = 16 * MB,
              WS_YT = 18 * MB, WS_WQP = 20 * MB, WS_WOP = WS_WQP + 1536 * 1024, WS_X2S = 22 * MB, WS_CS = WS_X2S + 256 * 1024;
constexpr int WS_GRAN = WS_CS + 16384, FL_O = WS_GRAN + 256 * 1024, FL_K = FL_O + 2 * 2048 * 128,
              FL_Y = FL_K + 256 * 128, FL_1 = FL_Y + 256 * 128, FL_U = FL_1 + 256 * 128, WS_EPOCH = FL_U + 64 * 128,
              WS_END = WS_EPOCH + 128;
constexpr unsigned BLANK = 0xffffffffu;
constexpr float FALLBACK_LIMIT = 1e30f;
constexpr unsigned SPIN_CAP = 1u << 17;
constexpr float QSCALE = 0.25f * 1.4426950408889634f;

constexpr int ATT_ONES = 131072, CV_H = ATT_ONES + 8192, CV_HS = 4160, CV_U = 4 * CV_HS, CV_IMG = CV_U + 2304 * 2;
constexpr int CT = CV_H + CV_IMG;
constexpr int CT_CV = CT, CT_K = CT + 4, CT_1 = CT + 12, KV_CNT = CT + 16, KV_POST = KV_CNT + 80, CT_Y = KV_POST + 16,
              CT_W = CT_Y + 4, CT_GO = CT_Y + 8, CT_AT = KV_POST + 4, CT_PG = KV_POST + 12, CV_PAIR = CT_Y + 16, OP_ST = CV_PAIR + 32, ATT_LDS = OP_ST + 1536;
constexpr int OP_Y = CV_H, OP_W = CV_H + 8192;
constexpr int CV_B1 = 98304, CV_RED = 81920;
constexpr int MG_O = 16384, MG_W = 49152;

#define MFMA16(a, b, c) __builtin_amdgcn_mfma_f32_16x16x32_bf16(asb(a), asb(b), (c), 0, 0, 0)
#define MFMA32(a, b, c) __builtin_amdgcn_mfma_f32_32x32x16_bf16(asb(a), asb(b), (c), 0, 0, 0)
#define SB() __builtin_amdgcn_sched_barrier(0)
#define PIN(x) asm volatile("" : "+v"(x))
#define FENCE() asm volatile("" ::: "memory")
#define WAIT_BAR(N) asm volatile("s_waitcnt vmcnt(" #N ") lgkmcnt(0)\n\ts_barrier" ::: "memory")
#define WAIT_VM(N) asm volatile("s_waitcnt vmcnt(" #N ")" ::: "memory")
#define WAIT_LGKM() asm volatile("s_waitcnt lgkmcnt(0)" ::: "memory")
#define BAR_L() asm volatile("s_waitcnt lgkmcnt(0)\n\ts_barrier" ::: "memory")

__device__ __forceinline__ float u2f(unsigned u) { return __builtin_bit_cast(float, u); }
__device__ __forceinline__ unsigned f2u(float f) { return __builtin_bit_cast(unsigned, f); }
__device__ __forceinline__ u64 ballot(bool p) { return __builtin_amdgcn_ballot_w64(p); }
__device__ __forceinline__ bool all(bool p) { return __builtin_amdgcn_ballot_w64(!p) == 0; }
__device__ __forceinline__ bool any(bool p) { return __builtin_amdgcn_ballot_w64(p) != 0; }
__device__ __forceinline__ bf16x8 asb(u32x4 v) { return __builtin_bit_cast(bf16x8, v); }
__device__ __forceinline__ unsigned pk2(float lo, float hi) {
  f32x2 f = {lo, hi};
  return __builtin_bit_cast(unsigned, __builtin_convertvector(f, bf16x2));
}
__device__ __forceinline__ float blo(unsigned u) { return u2f(u << 16); }
__device__ __forceinline__ float bhi(unsigned u) { return u2f(u & 0xffff0000u); }
__device__ __forceinline__ int uni(int x) { return __builtin_amdgcn_readfirstlane(x); }
__device__ __forceinline__ int lane_id() {
  int lane;
  asm volatile("v_mbcnt_lo_u32_b32 %0, -1, 0\n\tv_mbcnt_hi_u32_b32 %0, -1, %0" : "=v"(lane));
  return lane;
}
template <typename T> __device__ __forceinline__ LAS T& L(unsigned a) { return *(LAS T*)(uintptr_t)a; }
template <typename T> __device__ __forceinline__ volatile LAS T& LV(unsigned a) { return *(volatile LAS T*)(uintptr_t)a; }
__device__ __forceinline__ u32x2 trread(unsigned a) {
  return __builtin_bit_cast(u32x2, __builtin_amdgcn_ds_read_tr16_b64_v4i16((LAS s16x4*)(uintptr_t)a));
}
__device__ __forceinline__ void lds_add(unsigned a, unsigned v) { asm volatile("ds_add_u32 %0, %1" ::"v"(a), "v"(v) : "memory"); }
__device__ __forceinline__ unsigned lds_add_ret(unsigned a, unsigned v) {
  unsigned r;
  asm volatile("ds_add_rtn_u32 %0, %1, %2\n\ts_waitcnt lgkmcnt(0)" : "=v"(r) : "v"(a), "v"(v) : "memory");
  return r;
}
__device__ __forceinline__ void lds_wait(unsigned a, unsigned target) {
  while ((unsigned)uni(LV<unsigned>(a)) < target) __builtin_amdgcn_s_sleep(1);
  FENCE();
}
__device__ __forceinline__ rsrc_t mkrs(const void* p, unsigned bytes) {
  return __builtin_amdgcn_make_buffer_rsrc((void*)p, 0, bytes, 0x00020000);
}
__device__ __forceinline__ u32x4 mksrd(const void* p, unsigned bytes) {
  return u32x4{(unsigned)(uintptr_t)p, (unsigned)((uintptr_t)p >> 32) & 0xffffu, bytes, 0x00020000u};
}
#define DMA_HEAD "s_mov_b32 m0, %0\n\ts_nop 3\n\t"
#define DMA_OP(off, sc) "buffer_load_dwordx4 %1, %2, %3 offen offset:" #off sc " lds\n\t"
#define DMA_ARGS ::"s"(lds), "v"(voff), "s"(srd), "s"(soff) : "memory"
template <int N, bool DEV>
__device__ __forceinline__ void dma(u32x4 srd, unsigned voff, unsigned soff_, unsigned lds_) {
  const unsigned soff = uni(soff_), lds = uni(lds_);
  if constexpr (DEV) {
    if constexpr (N == 1) asm volatile(DMA_HEAD DMA_OP(0, " sc1") DMA_ARGS);
    if constexpr (N == 2) asm volatile(DMA_HEAD DMA_OP(0, " sc1") DMA_OP(1024, " sc1") DMA_ARGS);
    if constexpr (N == 3) asm volatile(DMA_HEAD DMA_OP(0, " sc1") DMA_OP(1024, " sc1") DMA_OP(2048, " sc1") DMA_ARGS);
  } else {
    if constexpr (N == 1) asm volatile(DMA_HEAD DMA_OP(0, "") DMA_ARGS);
    if constexpr (N == 4) asm volatile(DMA_HEAD DMA_OP(0, "") DMA_OP(1024, "") DMA_OP(2048, "") DMA_OP(3072, "") DMA_ARGS);
  }
}
__device__ __forceinline__ void st16(rsrc_t r, unsigned v, unsigned s, u32x4 d) { __builtin_amdgcn_raw_buffer_store_b128(d, r, v + s, 0, 16); }
__device__ __forceinline__ void st8(rsrc_t r, unsigned v, unsigned s, u32x2 d) { __builtin_amdgcn_raw_buffer_store_b64(d, r, v, s, 16); }
__device__ __forceinline__ u32x4 ld16(rsrc_t r, unsigned v, unsigned s) { return __builtin_amdgcn_raw_buffer_load_b128(r, v, s, 16); }
__device__ __forceinline__ u32x2 ld8(rsrc_t r, unsigned v, unsigned s) { return __builtin_amdgcn_raw_buffer_load_b64(r, v, s, 16); }

__device__ __forceinline__ unsigned umax3(unsigned a, unsigned b, unsigned c) { return max(max(a, b), c); }
__device__ __forceinline__ unsigned umax5(unsigned m, u32x4 v) { return umax3(umax3(m, v[0], v[1]), v[2], v[3]); }
__device__ __forceinline__ u32x4 noblank(u32x4 v) { return u32x4{min(v[0], BLANK - 1), min(v[1], BLANK - 1), min(v[2], BLANK - 1), min(v[3], BLANK - 1)}; }

struct Ctx {
  rsrc_t W;
  u32x4 Wd;
  u32x2 tag;
  int wid;
};

__device__ __forceinline__ void set_flag_last(const Ctx& c, unsigned off, unsigned ctr, unsigned last) {
  WAIT_VM(0);
  if (lane_id() == 0) {
    if (lds_add_ret(ctr, 1u) == last) st8(c.W, 0, off, c.tag);
  }
}
__device__ __forceinline__ u64 poll_flags(const Ctx& c, unsigned voff, unsigned off) {
  const u32x2 w = ld8(c.W, voff, off);
  const u64 m = ballot(w[0] == c.tag[0] && w[1] == c.tag[1]);
  FENCE();
  return m;
}
__device__ __forceinline__ void wait_flags(const Ctx& c, unsigned voff, unsigned off, int nap) {
#pragma unroll 1
  for (unsigned spins = 0; spins < SPIN_CAP; spins++) {
    if (poll_flags(c, voff, off) == ~0ull) break;
    if (nap == 1) __builtin_amdgcn_s_sleep(8);
    if (nap == 2) __builtin_amdgcn_s_sleep(2);
  }
}
__device__ __forceinline__ void poll4(const Ctx& c, u32x4 (&d)[4], unsigned v, unsigned s, unsigned step) {
#pragma unroll
  for (int i = 0; i < 4; i++) d[i] = ld16(c.W, v + (i & 1) * 1024, s + (i >> 1) * step);
#pragma unroll 1
  for (unsigned spins = 0; spins < SPIN_CAP; spins++) {
    bool bad[4];
#pragma unroll
    for (int i = 0; i < 4; i++) bad[i] = any(umax5(0u, d[i]) == BLANK);
    if (!(bad[0] || bad[1] || bad[2] || bad[3])) break;
    __builtin_amdgcn_s_sleep(4);
    FENCE();
#pragma unroll
    for (int i = 0; i < 4; i++)
      if (bad[i]) d[i] = ld16(c.W, v + (i & 1) * 1024, s + (i >> 1) * step);
  }
}
__device__ __forceinline__ float xor16add(float a) {
  auto r = __builtin_amdgcn_permlane16_swap(f2u(a), f2u(a), false, false);
  return u2f(r[0]) + u2f(r[1]);
}
__device__ __forceinline__ float xor32add(float a) {
  auto r = __builtin_amdgcn_permlane32_swap(f2u(a), f2u(a), false, false);
  return u2f(r[0]) + u2f(r[1]);
}
__device__ __forceinline__ float xor32max(float a) {
  auto r = __builtin_amdgcn_permlane32_swap(f2u(a), f2u(a), false, false);
  return fmaxf(u2f(r[0]), u2f(r[1]));
}
__device__ __forceinline__ float stat4(float s0, float s1, float q0, float q1) {
  const auto a = __builtin_amdgcn_permlane16_swap(f2u(s0), f2u(s1), false, false);
  const auto b = __builtin_amdgcn_permlane16_swap(f2u(q0), f2u(q1), false, false);
  const auto r = __builtin_amdgcn_permlane32_swap(f2u(u2f(a[0]) + u2f(a[1])),
                                                  f2u(u2f(b[0]) + u2f(b[1])), false, false);
  return u2f(r[0]) + u2f(r[1]);
}
__device__ __forceinline__ u32x4 frag16(f32x4 u, f32x4 v) {
  const auto lo = __builtin_amdgcn_permlane16_swap(pk2(u[0], u[1]), pk2(v[0], v[1]), false, false);
  const auto hi = __builtin_amdgcn_permlane16_swap(pk2(u[2], u[3]), pk2(v[2], v[3]), false, false);
  return u32x4{lo[0], hi[0], lo[1], hi[1]};
}

__device__ __forceinline__ void conv_stage_h(int c0, int a, const u16* ssmk) {
  const u32x4 hd = mksrd(ssmk, D * S * 2);
  const unsigned v = lane_id() * 16 + a * 2 + c0 * (S * 2);
  dma<4, false>(hd, v, 0, CV_H + a * CV_HS + 32);
  dma<4, false>(hd, v + S * 2, 0, CV_B1 + a * CV_HS + 32);
}
__device__ __forceinline__ void conv_stage_front(int c0, int a, const u16* ssmk) {
  const int lane = lane_id(), l = lane & 31, e = l + a - 16;
  const unsigned img = lane >> 5 ? CV_B1 : CV_H;
  const unsigned v = __builtin_amdgcn_raw_buffer_load_b16(mkrs(ssmk, D * S * 2), e >= 0 ? ((c0 + (lane >> 5)) * S + e) * 2 : -2, 0, 0);
  if (l < 8) L<u32x4>(img + CV_U + 4096 + a * 128 + l * 16) = u32x4{0u, 0u, 0u, 0u};
  if (l < 16) L<u16>(img + a * CV_HS + l * 2) = (u16)v;
}
__device__ __forceinline__ void conv_issue_u(const Ctx& c, int c0, int a) {
  const unsigned v = lane_id() * 16;
  dma<1, true>(c.Wd, v, WS_UREV + c0 * (S * 2) + a * 1024, CV_H + CV_U + a * 1024);
  dma<1, true>(c.Wd, v, WS_UREV + c0 * (S * 2) + S * 2 + a * 1024, CV_B1 + CV_U + a * 1024);
}
template <int J>
__device__ __forceinline__ void conv_compute(const Ctx& c, int ch, float dc, int w, unsigned img) {
  const int lane = lane_id(), n = lane & 15, g = lane >> 4, par = (w >> 1) & 1;
  const int sh = (n + 1) & 3;
  const unsigned ap = img + sh * CV_HS + (n + 1 - sh + 8 * g) * 2 + par * 64;
  const unsigned bp = img + CV_U + (2032 - 256 * J - 16 * n + 8 * g) * 2 + par * 64;
  f32x4 acc[4] = {};
  u32x2 A[2][J + 1][2];
  u32x4 B[2][J + 1];
  auto rd = [&](int st, int k, int r) {
    const int q = k / 3, e = k % 3;
    if (e < 2) A[st][q][e] = LV<u32x2>(ap + r * 128 + q * 512 + e * 8);
    else B[st][J - q] = L<u32x4>(bp + r * 128 + q * 512);
  };
  auto mf = [&](int st, int i) {
    const int q = i <= J ? 0 : i <= 2 * J ? 1 : i <= J + 9 ? 2 : 3;
    const int u = q == 0 ? i : q == 1 ? i - J - 1 : q == 2 ? i - 2 * J - 1 : i - J - 10;
    const int jt = q == 0 ? J : q == 1 ? J - 1 : q == 2 ? 8 - J : 7 - J;
    acc[q] = MFMA16((u32x4{A[st][u][0][0], A[st][u][0][1], A[st][u][1][0], A[st][u][1][1]}), B[st][jt - u], acc[q]);
  };
#pragma unroll
  for (int k = 0; k < 3 * (J + 1); k++) rd(0, k, 0);
  SB();
#pragma unroll
  for (int r = 0; r < 4; r++) {
#pragma unroll
    for (int i = 0; i < 18; i++) {
      mf(r & 1, i);
      SB();
      if (r < 3) {
        if (2 * i < 3 * (J + 1)) rd((r + 1) & 1, 2 * i, r + 1);
        if (2 * i + 1 < 3 * (J + 1)) rd((r + 1) & 1, 2 * i + 1, r + 1);
        SB();
      }
    }
  }
  const unsigned red = CV_RED + lane * 16;
  L<f32x4>(red + w * 2048) = par ? acc[0] : acc[2];
  L<f32x4>(red + w * 2048 + 1024) = par ? acc[1] : acc[3];
  const int t0 = 256 * (par ? 8 - J : J) + 16 * n + 4 * g;
  const u32x2 u0 = L<u32x2>(img + CV_U + (2044 - t0) * 2), u1 = L<u32x2>(img + CV_U + (2044 + 256 - t0) * 2);
  WAIT_LGKM();
  if (lane == 0) LV<unsigned>(CV_PAIR + w * 4) = 1u;
  lds_wait(CV_PAIR + (w ^ 2) * 4, 1);
  const f32x4 s0 = (par ? acc[2] : acc[0]) + L<f32x4>(red + (w ^ 2) * 2048);
  const f32x4 s1 = (par ? acc[3] : acc[1]) + L<f32x4>(red + (w ^ 2) * 2048 + 1024);
  st8(c.W, t0 * 2, WS_YT + ch * (S * 2),
      u32x2{pk2(fmaf(bhi(u0[1]), dc, s0[0]), fmaf(blo(u0[1]), dc, s0[1])), pk2(fmaf(bhi(u0[0]), dc, s0[2]), fmaf(blo(u0[0]), dc, s0[3]))});
  st8(c.W, t0 * 2, WS_YT + ch * (S * 2) - 512,
      u32x2{pk2(fmaf(bhi(u1[1]), dc, s1[0]), fmaf(blo(u1[1]), dc, s1[1])), pk2(fmaf(bhi(u1[0]), dc, s1[2]), fmaf(blo(u1[0]), dc, s1[3]))});
}

__device__ __forceinline__ u32x2 gemm1_load(const Ctx& c, const u16* x, const u16* wqkv, const u16* ssmk, int b, int rt, int ct, int a) {
  const int lane = lane_id(), n16 = lane & 15, g = lane >> 4;
  const u32x4 xd = mksrd(x, S * D * 2), wd = mksrd(wqkv, D * 3 * D * 2);
  const unsigned xv = (lane >> 3) * 1024 + (((lane & 7) ^ (lane >> 3)) & 7) * 16;
  const unsigned wv0 = g * 3072 + ((((n16 >> 1) ^ g) << 1) | (n16 & 1)) * 16, wv1 = (wv0 ^ 128) + 8 * 3072;
  const unsigned xs = (rt * 128 + a * 32) * 1024, wsb = a * 16 * 3072 + ct * 256;
  int kc = 0, kn = 1 + ((rt + ct) & 3) * 2;
  unsigned dst = a * 4096;
#pragma unroll 1
  for (int i = 0; i < 8; i++) {
    if (i >= 2) WAIT_BAR(8);
#pragma unroll
    for (int j = 0; j < 4; j++) dma<1, false>(xd, xv, xs + kc * 128 + j * 8192, dst + j * 1024);
#pragma unroll
    for (int j = 0; j < 4; j++) dma<1, false>(wd, j >> 1 ? wv1 : wv0, wsb + kc * (64 * 3072) + (j & 1) * 4 * 3072, dst + 16384 + j * 1024);
    kc = kn;
    kn = kn == 7 ? 1 : kn + 1;
    dst = dst >= 65536 ? dst - 65536 : dst + 32768;
  }
  conv_stage_h(b * 2, a, ssmk);
  WAIT_BAR(16);
  u32x2 w;
  asm volatile("s_nop 4\n\tbuffer_load_dwordx2 %0, %1, %2, %3 offen sc1\n\ts_waitcnt vmcnt(9)\n\ts_barrier\n\ts_waitcnt vmcnt(0)"
               : "=&v"(w) : "v"((lane & 7) * 1024), "s"(c.Wd), "s"(FL_U + (b >> 5) * 128) : "memory");
  return w;
}
template <bool VFORM>
__device__ __forceinline__ void gemm1(const Ctx& c, int rt, int ct) {
  const int lane = lane_id(), wid = c.wid;
  const int n16 = lane & 15, g = lane >> 4, tg = wid >> 1, fh = wid & 1;
  const unsigned ONE2 = 0x3f803f80u;
  const u32x4 ones = {ONE2, ONE2, ONE2, ONE2};
  const int q4 = n16 >> 2, p4 = n16 & 3, rkey = q4 | ((g & 1) << 2);
  unsigned wb[4], xr[2][2];
#pragma unroll
  for (int f = 0; f < 4; f++) wb[f] = 16384 + (8 * g + q4) * 256 + (((fh * 4 + ((f + tg) & 3)) ^ rkey) * 32) + p4 * 8;
#pragma unroll
  for (int t = 0; t < 2; t++)
#pragma unroll
    for (int k2 = 0; k2 < 2; k2++) xr[t][k2] = (tg * 32 + (t ^ fh) * 16 + n16) * 128 + (((4 * k2 + g) ^ n16) & 7) * 16;
  f32x4 acc[2][4] = {}, csum = {}, xs = {}, xq = {};
  u32x4 xf[2][2] = {};
  u32x2 wh[2][8] = {};
  auto mf = [&](int k2, int i) {
    const int t = (i >> 2) & 1, f = i & 3;
    const u32x4 w = {wh[k2][2 * f][0], wh[k2][2 * f][1], wh[k2][2 * f + 1][0], wh[k2][2 * f + 1][1]};
    if (i < 8) acc[t][f] = VFORM ? MFMA16(xf[k2][t], w, acc[t][f]) : MFMA16(w, xf[k2][t], acc[t][f]);
    else if (i == 8) csum = MFMA16(ones, w, csum);
    else if (i == 9) xs = MFMA16(ones, xf[k2][0], xs);
    else xq = MFMA16(xf[k2][0], xf[k2][0], xq);
  };
  auto step = [&](int kr) {
#pragma unroll
    for (int i = 0; i < 11; i++) {
      mf(kr ^ 1, i);
      SB();
      if (i == 0) xf[kr][0] = L<u32x4>(xr[0][kr]);
      if (i < 4) {
        wh[kr][2 * i] = trread(wb[i] + kr * 8192);
        wh[kr][2 * i + 1] = trread(wb[i] + kr * 8192 + 1024);
      }
      if (i == 4) xf[kr][1] = L<u32x4>(xr[1][kr]);
      SB();
    }
  };
  const int part = ct >> 2, h0 = (ct & 3) * 8 + fh * 4, tile = rt * 4 + tg;
  const unsigned dbase = part == 0 ? WS_QF + (h0 * 64 + tile) * 1024
                                   : WS_KVF + (h0 * 8 + (tile >> 3)) * 16384 + (part - 1) * 8192 + (tile & 7) * 1024;
  const int hs = part == 0 ? 65536 : 131072;
  unsigned lo = VFORM ? ((g & 1) ^ fh) * 512 + (g >> 1) * 256 + n16 * 16 : (lane ^ (fh * 16)) * 16;
  unsigned sw = OP_ST + 512 + (wid * 16 + n16) * 4, cw = OP_ST + ((fh * 4 + tg) * 16 + n16) * 4;
  PIN(lo); PIN(sw); PIN(cw);
#pragma unroll 1
  for (int k = 0; k < 8; k++) {
    BAR_L();
    step(0);
    step(1);
    const int adv = (k == 2 || k == 5) ? -65536 : 32768;
#pragma unroll
    for (int f = 0; f < 4; f++) wb[f] += adv;
#pragma unroll
    for (int i = 0; i < 4; i++) xr[i >> 1][i & 1] += adv;
  }
#pragma unroll
  for (int i = 10; i >= 0; i--) mf(1, i);
  SB();
  {
    float q0 = xq[0], q1 = xq[1], q2 = xq[2], q3 = xq[3];
    PIN(q0); PIN(q1); PIN(q2); PIN(q3);
    const float sq = (p4 & 2) ? ((p4 & 1) ? q3 : q2) : ((p4 & 1) ? q1 : q0);
    const float mean = xs[0] * (1.f / D);
    const float rstd = __builtin_amdgcn_rsqf(fmaxf(sq * (1.f / D) - mean * mean, 0.f) + 1e-5f) * (part == 0 ? QSCALE : 1.f);
    if (g == q4) {
      L<float>(cw) = csum[0];
      L<float>(sw) = -mean * rstd;
      L<float>(sw + 512) = rstd;
    }
  }
  BAR_L();
  f32x4 m4[2], r4[2];
#pragma unroll
  for (int t = 0; t < 2; t++) {
    const unsigned ms = OP_ST + 512 + (tg * 32 + (t ^ fh) * 16) * 4;
    if (VFORM) {
      m4[t] = L<f32x4>(ms + 16 * g);
      r4[t] = L<f32x4>(ms + 512 + 16 * g);
    } else {
      const float m = L<float>(ms + n16 * 4), r = L<float>(ms + 512 + n16 * 4);
      m4[t] = f32x4{m, m, m, m};
      r4[t] = f32x4{r, r, r, r};
    }
  }
#pragma unroll
  for (int f = 0; f < 4; f++) {
    const int ff = (f + tg) & 3;
    f32x4 c4;
    if (VFORM) {
      const float q = L<float>(OP_ST + ((fh * 4 + ff) * 16 + n16) * 4);
      c4 = f32x4{q, q, q, q};
    } else {
      c4 = L<f32x4>(OP_ST + (fh * 4 + ff) * 64 + 16 * g);
    }
    st16(c.W, lo, dbase + ff * hs, frag16(acc[0][f] * r4[0] + m4[0] * c4, acc[1][f] * r4[1] + m4[1] * c4));
  }
  if (rt == 0 && g == 0)
    __builtin_amdgcn_raw_buffer_store_b32(f2u(csum[0]), c.W, n16 * 4, WS_CS + ((h0 + tg) * 3 + part) * 128, 16);
}

__device__ __forceinline__ u32x4 qkv2_phase(const Ctx& c, int h, int panel) {
  const int lane = lane_id(), wid = c.wid;
  const int n16 = lane & 15, g = lane >> 4;
  const int rg = panel * 8 + wid;
  const f32x4 cq = __builtin_bit_cast(f32x4, ld16(c.W, g * 16, WS_CS + h * 384)), ck = __builtin_bit_cast(f32x4, ld16(c.W, g * 16, WS_CS + h * 384 + 128));
  const float csv = u2f(__builtin_amdgcn_raw_buffer_load_b32(c.W, n16 * 4, WS_CS + h * 384 + 256, 16));
  const unsigned l16 = lane * 16, xs = WS_X2F + rg * 32768;
  u32x4 ps[4];
  __builtin_amdgcn_s_sleep(24);
  poll4(c, ps, l16, WS_X2S + rg * 4096, 2048);
  FENCE();
  constexpr int WIN = 3;
  u32x4 xw[WIN][4];
  auto xload = [&](int k) {
#pragma unroll
    for (int i = 0; i < 4; i++) xw[k % WIN][i] = ld16(c.W, l16 + (i >> 1) * 1024, xs + (i & 1) * 16384 + k * 2048);
  };
  float rstd[2];
  f32x4 r4[2], aq[2], ak[2], av[2];
#pragma unroll 1
  for (unsigned spins = 0; spins < SPIN_CAP; spins++) {
    unsigned mx = 0;
#pragma unroll
    for (int k = 0; k < WIN; k++) xload(k);
    SB();
    {
      f32x4 e = (__builtin_bit_cast(f32x4, ps[0]) + __builtin_bit_cast(f32x4, ps[1])) + (__builtin_bit_cast(f32x4, ps[2]) + __builtin_bit_cast(f32x4, ps[3]));
#pragma unroll
      for (int i = 0; i < 4; i++) e[i] = xor32add(xor16add(e[i])) * (1.f / D);
      const unsigned sb = 7 * 16384 + 8192 + wid * 256;
      if (g == 0) L<f32x4>(sb + n16 * 16) = e;
      WAIT_LGKM();
#pragma unroll
      for (int t = 0; t < 2; t++) {
        const float mean = L<float>(sb + (t * 16 + n16) * 4), msq = L<float>(sb + 128 + (t * 16 + n16) * 4);
        const f32x4 m4 = L<f32x4>(sb + t * 64 + g * 16), q4 = L<f32x4>(sb + 128 + t * 64 + g * 16);
        rstd[t] = __builtin_amdgcn_rsqf(fmaxf(msq - mean * mean, 0.f) + 1e-5f);
#pragma unroll
        for (int i = 0; i < 4; i++) r4[t][i] = __builtin_amdgcn_rsqf(fmaxf(q4[i] - m4[i] * m4[i], 0.f) + 1e-5f);
        aq[t] = -mean * cq;
        ak[t] = -mean * ck;
        av[t] = -csv * m4;
        PIN(r4[t]);
      }
    }
    SB();
#pragma unroll
    for (int k = 0; k < 8; k++) {
#pragma unroll
      for (int k2 = 0; k2 < 2; k2++) {
        const unsigned wa = MG_W + l16 + (2 * k + k2) * 3072;
        const u32x4 w0 = L<u32x4>(wa), w1 = L<u32x4>(wa + 1024), w2 = L<u32x4>(wa + 2048);
#pragma unroll
        for (int t = 0; t < 2; t++) {
          const u32x4 xv = xw[k % WIN][k2 * 2 + t];
          aq[t] = MFMA16(w0, xv, aq[t]);
          ak[t] = MFMA16(w1, xv, ak[t]);
          av[t] = MFMA16(xv, w2, av[t]);
          mx = umax5(mx, xv);
        }
      }
      PIN(mx);
      SB();
      if (k + WIN < 8) xload(k + WIN);
      SB();
    }
    if (all(mx != BLANK)) break;
    FENCE();
  }
  const u32x4 ok = frag16(ak[0] * rstd[0], ak[1] * rstd[1]), ov = frag16(av[0] * r4[0], av[1] * r4[1]);
  const unsigned vo = 8192 + (g & 1) * 512 + (g >> 1) * 256 + n16 * 16, ks = WS_KV2 + (h * 8 + panel) * 16384 + wid * 1024;
  st16(c.W, l16, ks, ok);
  st16(c.W, vo, ks, ov);
  L<u32x4>(wid * 1024 + l16) = ok;
  L<u32x4>(wid * 1024 + vo) = ov;
  return frag16(aq[0] * (rstd[0] * QSCALE), aq[1] * (rstd[1] * QSCALE));
}

constexpr int PR_TS = 528;
constexpr int PR_SH = 64 * PR_TS, PR_W = PR_SH + 512;
__device__ __forceinline__ void prep_kv_load(int e, const u16* bk, const u16* bv, const u16* shk, u32x4 (&kr)[7], u32x4 (&vr)[4]) {
  const int tid = threadIdx.x;
  const rsrc_t kd = mkrs(bk, S * D * 2), vd = mkrs(bv, S * D * 2);
  int off = ((e >> 3) * 256 + 4 * (tid >> 3)) * 1024 + (e & 7) * 128 + (tid & 7) * 16;
  PIN(off);
  if (tid < 32) dma<1, false>(mksrd(shk, D * 8), tid * 16, (e & 7) * 512, PR_SH);
#pragma unroll
  for (int j = 0; j < 4; j++) kr[j] = __builtin_amdgcn_raw_buffer_load_b128(kd, off + (3 - j) * 1024, 0, 0);
#pragma unroll
  for (int j = 0; j < 4; j++) vr[j] = __builtin_amdgcn_raw_buffer_load_b128(vd, off + (3 - j) * 1024, 0, 0);
#pragma unroll
  for (int j = 4; j < 7; j++) {
    int o = off + (3 - j) * 1024;
    PIN(o);
    kr[j] = __builtin_amdgcn_raw_buffer_load_b128(kd, o, 0, 0);
  }
}
__device__ __forceinline__ void prep_kv(const Ctx& c, int e, const u32x4 (&kr)[7], const u32x4 (&vr)[4]) {
  const int tid = threadIdx.x, r4 = tid >> 3, ch = tid & 7;
  WAIT_BAR(0);
#pragma unroll
  for (int q = 0; q < 8; q++) {
    const u32x2 sk = L<u32x2>(PR_SH + (ch * 8 + q) * 8);
    float kx[7], o[4];
#pragma unroll
    for (int j = 0; j < 7; j++) kx[j] = (q & 1) ? bhi(kr[j][q >> 1]) : blo(kr[j][q >> 1]);
#pragma unroll
    for (int j = 0; j < 4; j++) {
      const float a = fmaf(kx[j], blo(sk[0]), fmaf(kx[j + 1], bhi(sk[0]), fmaf(kx[j + 2], blo(sk[1]), kx[j + 3] * bhi(sk[1]))));
      o[j] = a * ((q & 1) ? bhi(vr[j][q >> 1]) : blo(vr[j][q >> 1]));
    }
    L<u32x2>((q * 8 + ch) * PR_TS + (252 - 4 * r4) * 2) = u32x2{pk2(o[0], o[1]), pk2(o[2], o[3])};
  }
  BAR_L();
#pragma unroll
  for (int j = 0; j < 4; j++) {
    const int q = (tid >> 5) + 16 * j;
    st16(c.W, q * (S * 2) + (tid & 31) * 16, WS_UREV + (e & 7) * 64 * (S * 2) + (1792 - (e >> 3) * 256) * 2,
         L<u32x4>(((q & 7) * 8 + (q >> 3)) * PR_TS + (tid & 31) * 16));
  }
}
__device__ __forceinline__ void prep_w_load(int e, int a, const u16* wo, const u16* wqkv) {
  const int lane = lane_id(), kk = e >> 2, cq = e & 3;
  const unsigned dst = PR_W + a * 8192;
  const u32x4 od = mksrd(wo, D * D * 2), qd = mksrd(wqkv, D * 3 * D * 2);
#pragma unroll
  for (int i = 0; i < 2; i++) dma<1, false>(od, (lane >> 2) * 1024 + (lane & 3) * 16, (kk * 32 + i * 16) * 1024 + cq * 256 + a * 64, dst + i * 1024);
#pragma unroll
  for (int i = 0; i < 6; i++)
    dma<1, false>(qd, (lane >> 2) * 3072 + (lane & 3) * 16, (kk * 32 + (i & 1) * 16) * 3072 + cq * 768 + a * 192 + (i >> 1) * 64,
                  dst + 2048 + i * 1024);
}
__device__ __forceinline__ void prep_w_store(const Ctx& c, int e, int a) {
  const int lane = lane_id(), kk = e >> 2, cq = e & 3, n = lane & 15, g = lane >> 4;
  const unsigned img = PR_W + a * 8192;
#pragma unroll
  for (int i = 0; i < 2; i++) {
    const unsigned p = img + (16 * (g >> 1) + 4 * (g & 1) + (n >> 2)) * 64 + (8 * (n & 3) + 4 * i) * 2;
    const u32x2 u = trread(p), v = trread(p + 8 * 64);
    st16(c.W, lane * 16, WS_WOP + (((cq * 4 + a) * 16 + kk) * 2 + i) * 1024, u32x4{u[0], u[1], v[0], v[1]});
  }
#pragma unroll
  for (int i = 0; i < 6; i++) {
    const int j = cq * 24 + a * 6 + i;
    const unsigned p = img + 2048 + (i >> 1) * 2048 + (8 * g + (n >> 2)) * 64 + (i & 1) * 32 + (n & 3) * 8;
    const u32x2 u = trread(p), v = trread(p + 4 * 64);
    st16(c.W, lane * 16, WS_WQP + ((j & 31) * 48 + kk * 3 + (j >> 5)) * 1024, u32x4{u[0], u[1], v[0], v[1]});
  }
}

__device__ __forceinline__ float max16(const f32x16& s) {
  float a = fmaxf(fmaxf(s[0], s[1]), s[2]), b = fmaxf(fmaxf(s[3], s[4]), s[5]);
  a = fmaxf(fmaxf(a, s[6]), s[7]); b = fmaxf(fmaxf(b, s[8]), s[9]);
  a = fmaxf(fmaxf(a, s[10]), s[11]); b = fmaxf(fmaxf(b, s[12]), s[13]);
  a = fmaxf(fmaxf(a, s[14]), s[15]);
  return fmaxf(a, b);
}
__device__ __forceinline__ void attn_loop(f32x16& o, float mx, u32x4 qv, u32x4 k0, u32x4 kf, u32x4 v0, unsigned kb, unsigned vb, unsigned vs,
                                          unsigned fa, unsigned da, unsigned dv, int yg, u32x4 srd, unsigned ho,
                                          u32x2 tag) {
  unsigned idx = 0xDDDD, ft;
  int p = 0;
  f32x16 sa, sb, nm;
  asm volatile(
      "s_cmp_eq_u32 %[yg], 0\n\ts_cbranch_scc1 3f\n\ts_setprio 1\n\t3:\n\tv_mov_b32 v80, %[mx]\n\t"
      "v_mov_b32 v81, %[mx]\n\tv_mov_b64 v[82:83], v[80:81]\n\tv_mov_b64 v[84:85], v[80:81]\n\t"
      "v_mov_b64 v[86:87], v[80:81]\n\tv_mov_b64 v[88:89], v[80:81]\n\tv_mov_b64 v[90:91], v[80:81]\n\t"
      "v_mov_b64 v[92:93], v[80:81]\n\tv_mov_b64 v[94:95], v[80:81]\n\ts_waitcnt lgkmcnt(0)\n\ts_nop 0\n\t"
      "v_mfma_f32_32x32x16_bf16 v[48:63], v[108:111], v[96:99], v[80:95]\n\tv_mov_b64 v[32:33], 0\n\t"
      "v_mov_b64 v[34:35], 0\n\tv_mov_b64 v[36:37], 0\n\tv_mov_b64 v[38:39], 0\n\tv_mov_b64 v[40:41], 0\n\t"
      "v_mov_b64 v[42:43], 0\n\tv_mov_b64 v[44:45], 0\n\tv_mov_b64 v[46:47], 0\n\ts_nop 4\n\tv_exp_f32 v48, v48\n\t"
      "v_exp_f32 v49, v49\n\ts_branch 10f\n\t1:\n\ts_cmp_eq_u32 %[yg], 0\n\ts_cbranch_scc1 4f\n\ts_setprio 1\n\t"
      "4:\n\tv_exp_f32 v48, v48\n\tv_exp_f32 v49, v49\n\ts_waitcnt lgkmcnt(2)\n\t"
      "v_smfmac_f32_32x32x32_bf16 v[32:47], v[108:111], v[72:79], %[idx]\n\t10:\n\t"
      "ds_read_b128 v[108:111], %[vb] offset:512\n\tv_exp_f32 v50, v50\n\tv_exp_f32 v51, v51\n\t"
      "v_exp_f32 v52, v52\n\tv_exp_f32 v53, v53\n\ts_waitcnt lgkmcnt(2)\n\t"
      "v_mfma_f32_32x32x16_bf16 v[64:79], v[100:103], v[96:99], v[80:95]\n\t"
      "ds_read_b128 v[100:103], %[kb] offset:2048\n\tv_exp_f32 v54, v54\n\tv_exp_f32 v55, v55\n\t"
      "v_exp_f32 v56, v56\n\tv_exp_f32 v57, v57\n\tv_exp_f32 v58, v58\n\ts_waitcnt lgkmcnt(2)\n\t"
      "v_smfmac_f32_32x32x32_bf16 v[32:47], v[104:107], v[48:55], %[idx]\n\t"
      "ds_read_b128 v[104:107], %[vb] offset:1024\n\tv_exp_f32 v59, v59\n\tv_exp_f32 v60, v60\n\t"
      "v_exp_f32 v61, v61\n\tv_exp_f32 v62, v62\n\tv_exp_f32 v63, v63\n\tv_exp_f32 v64, v64\n\tv_exp_f32 v65, v65\n\t"
      "s_waitcnt lgkmcnt(2)\n\tv_smfmac_f32_32x32x32_bf16 v[32:47], v[108:111], v[56:63], %[idx]\n\t"
      "ds_read_b128 v[108:111], %[vb] offset:1536\n\tv_exp_f32 v66, v66\n\tv_exp_f32 v67, v67\n\t"
      "v_exp_f32 v68, v68\n\tv_exp_f32 v69, v69\n\ts_waitcnt lgkmcnt(2)\n\t"
      "v_mfma_f32_32x32x16_bf16 v[48:63], v[100:103], v[96:99], v[80:95]\n\t"
      "ds_read_b128 v[100:103], %[kb] offset:3072\n\tv_exp_f32 v70, v70\n\tv_exp_f32 v71, v71\n\t"
      "v_exp_f32 v72, v72\n\tv_exp_f32 v73, v73\n\tv_exp_f32 v74, v74\n\ts_waitcnt lgkmcnt(2)\n\t"
      "v_smfmac_f32_32x32x32_bf16 v[32:47], v[104:107], v[64:71], %[idx]\n\t"
      "ds_read_b128 v[104:107], %[vb] offset:2048\n\tv_exp_f32 v75, v75\n\tv_exp_f32 v76, v76\n\t"
      "v_exp_f32 v77, v77\n\tv_exp_f32 v78, v78\n\tv_exp_f32 v79, v79\n\tv_exp_f32 v48, v48\n\tv_exp_f32 v49, v49\n\t"
      "s_waitcnt lgkmcnt(2)\n\tv_smfmac_f32_32x32x32_bf16 v[32:47], v[108:111], v[72:79], %[idx]\n\t"
      "ds_read_b128 v[108:111], %[vb] offset:2560\n\tv_exp_f32 v50, v50\n\tv_exp_f32 v51, v51\n\t"
      "v_exp_f32 v52, v52\n\tv_exp_f32 v53, v53\n\ts_waitcnt lgkmcnt(2)\n\t"
      "v_mfma_f32_32x32x16_bf16 v[64:79], v[100:103], v[96:99], v[80:95]\n\t"
      "ds_read_b128 v[100:103], %[kb] offset:4096\n\tv_exp_f32 v54, v54\n\tv_exp_f32 v55, v55\n\t"
      "v_exp_f32 v56, v56\n\tv_exp_f32 v57, v57\n\tv_exp_f32 v58, v58\n\ts_waitcnt lgkmcnt(2)\n\t"
      "v_smfmac_f32_32x32x32_bf16 v[32:47], v[104:107], v[48:55], %[idx]\n\t"
      "ds_read_b128 v[104:107], %[vb] offset:3072\n\tv_exp_f32 v59, v59\n\tv_exp_f32 v60, v60\n\t"
      "v_exp_f32 v61, v61\n\tv_exp_f32 v62, v62\n\tv_exp_f32 v63, v63\n\tv_exp_f32 v64, v64\n\tv_exp_f32 v65, v65\n\t"
      "s_waitcnt lgkmcnt(2)\n\tv_smfmac_f32_32x32x32_bf16 v[32:47], v[108:111], v[56:63], %[idx]\n\t"
      "ds_read_b128 v[108:111], %[vb] offset:3584\n\tv_exp_f32 v66, v66\n\tv_exp_f32 v67, v67\n\t"
      "v_exp_f32 v68, v68\n\tv_exp_f32 v69, v69\n\ts_waitcnt lgkmcnt(2)\n\t"
      "v_mfma_f32_32x32x16_bf16 v[48:63], v[100:103], v[96:99], v[80:95]\n\t"
      "ds_read_b128 v[100:103], %[kb] offset:5120\n\tv_exp_f32 v70, v70\n\tv_exp_f32 v71, v71\n\t"
      "v_exp_f32 v72, v72\n\tv_exp_f32 v73, v73\n\tv_exp_f32 v74, v74\n\ts_waitcnt lgkmcnt(2)\n\t"
      "v_smfmac_f32_32x32x32_bf16 v[32:47], v[104:107], v[64:71], %[idx]\n\t"
      "ds_read_b128 v[104:107], %[vb] offset:4096\n\tv_exp_f32 v75, v75\n\tv_exp_f32 v76, v76\n\t"
      "v_exp_f32 v77, v77\n\tv_exp_f32 v78, v78\n\tv_exp_f32 v79, v79\n\ts_cmp_eq_u32 %[p], 0\n\t"
      "s_cbranch_scc1 6f\n\t7:\n\tv_exp_f32 v48, v48\n\tv_exp_f32 v49, v49\n\ts_waitcnt lgkmcnt(2)\n\t"
      "v_smfmac_f32_32x32x32_bf16 v[32:47], v[108:111], v[72:79], %[idx]\n\t"
      "ds_read_b128 v[108:111], %[vb] offset:4608\n\tv_exp_f32 v50, v50\n\tv_exp_f32 v51, v51\n\t"
      "v_exp_f32 v52, v52\n\tv_exp_f32 v53, v53\n\ts_waitcnt lgkmcnt(2)\n\t"
      "v_mfma_f32_32x32x16_bf16 v[64:79], v[100:103], v[96:99], v[80:95]\n\t"
      "ds_read_b128 v[100:103], %[kb] offset:6144\n\tv_exp_f32 v54, v54\n\tv_exp_f32 v55, v55\n\t"
      "v_exp_f32 v56, v56\n\tv_exp_f32 v57, v57\n\tv_exp_f32 v58, v58\n\ts_waitcnt lgkmcnt(2)\n\t"
      "v_smfmac_f32_32x32x32_bf16 v[32:47], v[104:107], v[48:55], %[idx]\n\t"
      "ds_read_b128 v[104:107], %[vb] offset:5120\n\tv_exp_f32 v59, v59\n\tv_exp_f32 v60, v60\n\t"
      "v_exp_f32 v61, v61\n\tv_exp_f32 v62, v62\n\tv_exp_f32 v63, v63\n\tds_read_b32 %[ft], %[fa]\n\t"
      "v_exp_f32 v64, v64\n\tv_exp_f32 v65, v65\n\ts_waitcnt lgkmcnt(2)\n\t"
      "v_smfmac_f32_32x32x32_bf16 v[32:47], v[108:111], v[56:63], %[idx]\n\t"
      "ds_read_b128 v[108:111], %[vb] offset:5632\n\tv_exp_f32 v66, v66\n\tv_exp_f32 v67, v67\n\t"
      "v_exp_f32 v68, v68\n\tv_exp_f32 v69, v69\n\ts_waitcnt lgkmcnt(2)\n\t"
      "v_mfma_f32_32x32x16_bf16 v[48:63], v[100:103], v[96:99], v[80:95]\n\t"
      "ds_read_b128 v[100:103], %[kb] offset:7168\n\tv_exp_f32 v70, v70\n\tv_exp_f32 v71, v71\n\t"
      "v_exp_f32 v72, v72\n\tv_exp_f32 v73, v73\n\tv_exp_f32 v74, v74\n\ts_waitcnt lgkmcnt(2)\n\t"
      "v_smfmac_f32_32x32x32_bf16 v[32:47], v[104:107], v[64:71], %[idx]\n\t"
      "ds_read_b128 v[104:107], %[vb] offset:6144\n\ts_setprio 0\n\tv_exp_f32 v75, v75\n\tv_exp_f32 v76, v76\n\t"
      "v_exp_f32 v77, v77\n\tv_exp_f32 v78, v78\n\tv_exp_f32 v79, v79\n\tv_cmp_gt_u32 vcc, 8, %[ft]\n\t"
      "s_cbranch_vccnz 2f\n\t5:\n\tv_add_u32 %[fa], 4, %[fa]\n\tv_exp_f32 v48, v48\n\tv_exp_f32 v49, v49\n\t"
      "s_waitcnt lgkmcnt(2)\n\tv_smfmac_f32_32x32x32_bf16 v[32:47], v[108:111], v[72:79], %[idx]\n\t"
      "ds_read_b128 v[108:111], %[vb] offset:6656\n\tv_exp_f32 v50, v50\n\tv_exp_f32 v51, v51\n\t"
      "v_exp_f32 v52, v52\n\tv_exp_f32 v53, v53\n\ts_waitcnt lgkmcnt(2)\n\t"
      "v_mfma_f32_32x32x16_bf16 v[64:79], v[100:103], v[96:99], v[80:95]\n\t"
      "ds_read_b128 v[100:103], %[kb] offset:16384\n\tv_exp_f32 v54, v54\n\tv_exp_f32 v55, v55\n\t"
      "v_exp_f32 v56, v56\n\tv_exp_f32 v57, v57\n\tv_exp_f32 v58, v58\n\ts_waitcnt lgkmcnt(2)\n\t"
      "v_smfmac_f32_32x32x32_bf16 v[32:47], v[104:107], v[48:55], %[idx]\n\t"
      "ds_read_b128 v[104:107], %[vb] offset:7168\n\tv_exp_f32 v59, v59\n\tv_exp_f32 v60, v60\n\t"
      "v_exp_f32 v61, v61\n\tv_exp_f32 v62, v62\n\tv_exp_f32 v63, v63\n\ts_cmp_eq_u32 %[p], 7\n\t"
      "s_cbranch_scc1 8f\n\t11:\n\tv_exp_f32 v64, v64\n\tv_exp_f32 v65, v65\n\ts_waitcnt lgkmcnt(2)\n\t"
      "v_smfmac_f32_32x32x32_bf16 v[32:47], v[108:111], v[56:63], %[idx]\n\t"
      "ds_read_b128 v[108:111], %[vb] offset:7680\n\tv_exp_f32 v66, v66\n\tv_exp_f32 v67, v67\n\t"
      "v_exp_f32 v68, v68\n\tv_exp_f32 v69, v69\n\ts_waitcnt lgkmcnt(2)\n\t"
      "v_mfma_f32_32x32x16_bf16 v[48:63], v[100:103], v[96:99], v[80:95]\n\t"
      "ds_read_b128 v[100:103], %[kb] offset:17408\n\tv_exp_f32 v70, v70\n\tv_exp_f32 v71, v71\n\t"
      "v_exp_f32 v72, v72\n\tv_exp_f32 v73, v73\n\tv_exp_f32 v74, v74\n\ts_waitcnt lgkmcnt(2)\n\t"
      "v_smfmac_f32_32x32x32_bf16 v[32:47], v[104:107], v[64:71], %[idx]\n\tv_add_u32 %[vb], %[vb], %[vs]\n\t"
      "ds_read_b128 v[104:107], %[vb]\n\tv_exp_f32 v75, v75\n\tv_exp_f32 v76, v76\n\tv_exp_f32 v77, v77\n\t"
      "v_exp_f32 v78, v78\n\tv_exp_f32 v79, v79\n\tv_add_u32 %[kb], 0x4000, %[kb]\n\ts_add_u32 %[p], %[p], 1\n\t"
      "s_cmp_lg_u32 %[p], 8\n\ts_cbranch_scc1 1b\n\ts_branch 9f\n\t2:\n\tds_read_b32 %[ft], %[fa]\n\t"
      "s_waitcnt lgkmcnt(0)\n\tv_cmp_gt_u32 vcc, 8, %[ft]\n\ts_cbranch_vccz 5b\n\ts_sleep 1\n\ts_branch 2b\n\t6:\n\t"
      "s_cmp_eq_u32 %[p], 0\n\ts_cbranch_scc0 8f\n\ts_waitcnt vmcnt(0)\n\tds_add_u32 %[da], %[dv]\n\ts_branch 7b\n\t"
      "8:\n\tbuffer_store_dwordx2 %[tag], off, %[srd], %[ho] sc1\n\ts_branch 11b\n\t9:\n\ts_waitcnt lgkmcnt(0)\n\t"
      "v_smfmac_f32_32x32x32_bf16 v[32:47], v[108:111], v[72:79], %[idx]\n\ts_nop 7\n\ts_nop 7\n\t"
      : "={v[32:47]}"(o), "={v[48:63]}"(sa), "={v[64:79]}"(sb), "={v[80:95]}"(nm), "+{v[100:103]}"(kf), "+{v[104:107]}"(v0),
        "+{v[108:111]}"(k0), [kb] "+&v"(kb), [vb] "+&v"(vb), [p] "+&s"(p), [fa] "+&v"(fa), [ft] "=&v"(ft), [mx] "+&v"(mx),
        [vs] "+&v"(vs), [idx] "+&v"(idx), [da] "+&v"(da), [dv] "+&v"(dv), [tag] "+&v"(tag)
      : "{v[96:99]}"(qv), [yg] "s"(yg), [srd] "s"(srd), [ho] "s"(ho)
      : "memory", "scc", "vcc");
}
__device__ __forceinline__ u32x2 attn_main(const Ctx& c, u32x4 qv, int layer, int h, int panel, unsigned hs) {
  const int lane = lane_id(), wid = c.wid;
  const bool vrow = (lane & 16) == 0;
  const unsigned kb0 = lane * 16, cnt = KV_CNT + layer * 40 + 4;
  const unsigned vb0 = (vrow ? 8192 + (lane >> 5) * 256 : ATT_ONES) + (lane & 15) * 16;
  float m = xor32max(max16(MFMA32(L<u32x4>(kb0), qv, f32x16{})));
  if (layer) set_flag_last(c, FL_K + (h * 8 + panel) * 128, CT_K, 7);
  f32x16 o;
  u32x2 hw;
  unsigned pg;
  int pass = 0;
  asm volatile("" : "+s"(pass));
#pragma unroll 1
  for (;; pass++) {
    attn_loop(o, -m, qv, L<u32x4>(kb0), L<u32x4>(kb0 + 1024), L<u32x4>(vb0), kb0, vb0, vrow ? 16384 : 0, cnt, cnt + (lane & 1) * 4,
              (layer == 0 && lane < 2) ? 1u : 0u, wid >> 2, c.Wd, FL_O + ((layer * 64 + panel * 8 + wid) * 32 + h) * 128, c.tag);
    hw = ld8(c.W, (lane & 3) * 128, hs);
    pg = LV<unsigned>(CT_PG);
    SB();
    o[8] = xor32add(o[8]);
    if (pass == 1) break;
    float mag = o[8];
#pragma unroll
    for (int r = 0; r < 8; r++) mag += fabsf(o[r]);
    if (all(mag < FALLBACK_LIMIT)) break;
    float mm = m;
#pragma unroll 1
    for (int t = 0; t < 64; t++) mm = fmaxf(mm, max16(MFMA32(L<u32x4>(kb0 + (t >> 3) * 16384 + (t & 7) * 1024), qv, f32x16{})));
    m = xor32max(mm);
  }
  const float il = __builtin_amdgcn_rcpf(o[8]);
  const u32x4 ov = {pk2(o[0] * il, o[1] * il), pk2(o[2] * il, o[3] * il), pk2(o[4] * il, o[5] * il), pk2(o[6] * il, o[7] * il)};
  const int rgo = layer * 64 + panel * 8 + wid;
  if (uni(pg) < 4) lds_wait(CT_PG, 4);
  st16(c.W, (lane & 16) * 1024 + (lane >> 5) * 256 + (lane & 15) * 16, WS_OF + rgo * 32768 + h * 512, noblank(ov));
  SB();
  if (lane == 0) lds_add(CT_AT, 1u);
  PIN(hw);
  return hw;
}

constexpr int NWR = 28;
__device__ __forceinline__ void oproj_phase(const Ctx& c, int layer, const u32x4 (&wf)[NWR], f32x4 (&acc)[2][2], u16* out, int rg, int fq) {
  const int lane = lane_id();
  const int a = c.wid - 8, n16 = lane & 15, g = lane >> 4, fg = fq * 4 + a;
  const unsigned l16 = lane * 16;
  BAR_L();
  const unsigned ob = MG_O + l16, wl = OP_W + a * 3072 + l16;
  u32x4 of[4];
#pragma unroll
  for (int i = 0; i < 4; i++) of[i] = L<u32x4>(ob + (i & 1) * 16384 + (i >> 1) * 1024);
#pragma unroll
  for (int s = 0; s < 32; s++) {
    const int kk = s >> 1, t = s & 1, r = kk < 14 ? s & 3 : t;
    acc[t][0] = MFMA16(kk < 14 ? wf[kk < 14 ? kk * 2 : 0] : of[2], of[r], acc[t][0]);
    acc[t][1] = MFMA16(kk < 14 ? wf[kk < 14 ? kk * 2 + 1 : 0] : of[3], of[r], acc[t][1]);
    SB();
    if (s < 26) of[r] = L<u32x4>(ob + t * 16384 + (kk + 2) * 1024);
    if (s == 26) of[2] = L<u32x4>(ATT_ONES + a * 2048 + g * 512 + 256 + n16 * 16);
    if (s == 27) of[3] = L<u32x4>(wl);
    if (s == 28 || s == 29) of[t] = L<u32x4>(ob + t * 16384 + 15 * 1024);
    if (s == 29) { of[2] = L<u32x4>(wl + 1024); of[3] = L<u32x4>(wl + 2048); }
    SB();
  }
  float sa[2] = {0.f, 0.f}, sq[2] = {0.f, 0.f};
#pragma unroll
  for (int t = 0; t < 2; t++)
#pragma unroll
    for (int j = 0; j < 8; j++) {
      const float v = acc[t][j >> 2][j & 3];
      sa[t] += v;
      sq[t] = fmaf(v, v, sq[t]);
    }
  L<float>(OP_ST + a * 256 + lane * 4) = stat4(sa[0], sa[1], sq[0], sq[1]);
  BAR_L();
  if (a == 0) {
    const unsigned gs = WS_GRAN + (layer * 64 + rg) * 2048, tk = OP_ST + lane * 4, th = lane < 32 ? c.tag[0] : c.tag[1];
    const float p = (L<float>(tk) + L<float>(tk + 256)) + (L<float>(tk + 512) + L<float>(tk + 768));
    st8(c.W, lane * 8, gs + fq * 512, u32x2{f2u(p), th});
    __builtin_amdgcn_s_sleep(10);
    float u;
#pragma unroll 1
    for (unsigned spins = 0; spins < SPIN_CAP; spins++) {
      const u32x2 w0 = ld8(c.W, lane * 8, gs), w1 = ld8(c.W, lane * 8 + 512, gs), w2 = ld8(c.W, lane * 8 + 1024, gs), w3 = ld8(c.W, lane * 8 + 1536, gs);
      u = (u2f(w0[0]) + u2f(w1[0])) + (u2f(w2[0]) + u2f(w3[0]));
      if (all(w0[1] == th && w1[1] == th && w2[1] == th && w3[1] == th)) break;
      FENCE();
    }
    const auto r = __builtin_amdgcn_permlane32_swap(f2u(u), f2u(u), false, false);
    const float ua = u2f(r[0]) * (1.f / D), uq = u2f(r[1]) * (1.f / D);
    const float rstd = __builtin_amdgcn_rsqf(fmaxf(uq - ua * ua, 0.f) + 1e-5f);
    L<f32x2>(OP_ST + 1024 + (lane & 31) * 8) = f32x2{rstd, -ua * rstd};
  }
  const unsigned yb = OP_Y + a * 2048 + (8 * g + (n16 >> 2)) * 64 + (n16 & 3) * 8;
  u32x2 yv[2][2];
#pragma unroll
  for (int t = 0; t < 2; t++) {
    yv[t][0] = trread(yb + t * 32);
    yv[t][1] = trread(yb + t * 32 + 256);
  }
  BAR_L();
  u32x4 xr[2];
#pragma unroll
  for (int t = 0; t < 2; t++) {
    f32x2 rs = L<f32x2>(OP_ST + 1024 + (t * 16 + n16) * 8);
#pragma unroll
    for (int j = 0; j < 4; j++) {
      const float lo = acc[t][j >> 1][(j & 1) * 2], hi = acc[t][j >> 1][(j & 1) * 2 + 1];
      const unsigned y = yv[t][j >> 1][j & 1];
      acc[t][j >> 1][(j & 1) * 2] = fmaf(fmaf(lo, rs[0], rs[1]), blo(y), lo);
      acc[t][j >> 1][(j & 1) * 2 + 1] = fmaf(fmaf(hi, rs[0], rs[1]), bhi(y), hi);
      xr[t][j] = pk2(acc[t][j >> 1][(j & 1) * 2], acc[t][j >> 1][(j & 1) * 2 + 1]);
    }
    if (layer) st16(mkrs(out, S * D * 2), n16 * 1024 + g * 16, (rg * 32 + t * 16) * 1024 + fg * 64, xr[t]);
    else st16(c.W, l16, WS_X2F + rg * 32768 + (t * 16 + fg) * 1024, noblank(xr[t]));
  }
  if (layer == 0) {
    float ua[2] = {0.f, 0.f}, uq[2] = {0.f, 0.f};
#pragma unroll
    for (int t = 0; t < 2; t++)
#pragma unroll
      for (int j = 0; j < 4; j++) {
        const float l2 = blo(xr[t][j]), h2 = bhi(xr[t][j]);
        ua[t] += l2 + h2;
        uq[t] = fmaf(l2, l2, fmaf(h2, h2, uq[t]));
      }
    __builtin_amdgcn_raw_buffer_store_b32(min(f2u(stat4(ua[0], ua[1], uq[0], uq[1])), BLANK - 1), c.W, lane * 4, WS_X2S + rg * 4096 + fg * 256, 16);
  }
}

__device__ __forceinline__ void kv_fetch(const Ctx& c, bool SECOND, int h, int p0, int a) {
  const int lane = lane_id();
  const unsigned src = (SECOND ? WS_KV2 : WS_KVF) + h * 131072 + a * 2048, cnt = KV_CNT + (SECOND ? 40 : 0), post = KV_POST + (SECOND ? 8 : 0);
  const int pl = (p0 + lane) & 7;
  const unsigned fi = (SECOND ? h * 8 + pl : (2 * pl + ((lane >> 4) & 1)) * 12 + 4 + ((lane >> 1) & 4) + (h >> 3)) * 128;
  const int j0 = SECOND ? 1 : 3;
  int j = j0, told = j0;
  unsigned ready = 0u, seen = 0u, spins = 0u;
  auto tell = [&]() {
    if (lane == 0) lds_add(cnt + told * 4, 2u);
    ++told;
  };
#pragma unroll 1
  while (j < 8 || (a == 0 && seen != ~0u)) {
    if (((ready >> j) & 1u) == 0) {
      if (a == 0) {
        seen = (unsigned)poll_flags(c, fi, SECOND ? FL_K : FL_1);
        if (++spins >= SPIN_CAP) seen = ~0u;
        if (lane == 0) LV<unsigned>(post) = seen;
      } else {
        seen = uni(LV<unsigned>(post));
        __builtin_amdgcn_s_sleep(2);
      }
      const unsigned m = seen >> ((a >> 1) * 16);
      ready = m & (m >> 8) & 0xffu;
      WAIT_VM(0);
      while (told < j) tell();
      continue;
    }
    const unsigned p = src + ((p0 + j) & 7) * 16384, dst = j * 16384 + a * 2048;
    dma<2, true>(c.Wd, lane * 16, p, dst);
    dma<2, true>(c.Wd, lane * 16, p + 8192, dst + 8192);
    ++j;
    if (j - told == 3) {
      WAIT_VM(8);
      tell();
    } else if (j - told == 2 && told == j0) {
      WAIT_VM(4);
      tell();
    }
  }
  if (j - told == 2) {
    WAIT_VM(4);
    tell();
  }
  WAIT_VM(0);
  if (told < j) tell();
}

__global__ void __launch_bounds__(768, 3) k_mega(const u16* x, const u16* wqkv, const u16* wo, const u16* bk, const u16* bv,
                                                 const u16* shk, const u16* ssmk, const u16* ssmd, char* ws, u16* out) {
  const int b = blockIdx.x;
  const int h = ((b >> 2) & 1) * 16 + (b >> 4), panel = (b & 3) * 2 + ((b >> 3) & 1);
  const int rg = (((b & 3) * 2 + 4 + ((b >> 2) & 1)) & 7) * 8 + (b >> 5), fq = (b >> 3) & 3;
  const int rt = ((b & 7) >> 1) * 4 + ((b >> 3) & 3), ct = (b & 1) * 6 + (b >> 5);
  const int pfirst = b & 7;
  const int tid = threadIdx.x, wid = uni(tid >> 6);
  Ctx c;
  c.W = mkrs(ws, WS_END);
  c.Wd = mksrd(ws, WS_END);
  c.wid = wid;
  const u32x2 efound = ld8(c.W, 0, WS_EPOCH);
  if (tid >= 512 && tid < 552) L<unsigned>(CT + (tid - 512) * 4) = (tid == 524 || tid == 534) ? 8u : 0u;
  int role = uni((wid >> 3) + (b >= 192 ? 2 : 0));
  asm volatile("" : "+s"(role));
  u64 epoch = 0;
  auto read_epoch = [&]() {
    epoch = (((u64)(unsigned)uni((int)efound[1]) << 32) | (unsigned)uni((int)efound[0])) + 1ull;
    const u64 t = epoch * 0x9E3779B97F4A7C15ull;
    c.tag = u32x2{(unsigned)t, (unsigned)(t >> 32)};
    asm volatile("" : "+s"(c.tag));
  };
  bool uearly = false;
  if (role & 1) {
    u32x2 useen = {0u, 0u};
    if (role == 1) useen = gemm1_load(c, x, wqkv, ssmk, b, rt, ct, wid - 8);
    read_epoch();
    uearly = role == 1 && all(useen[0] == c.tag[0] && useen[1] == c.tag[1]);
    if (uearly) conv_issue_u(c, b * 2, wid - 8);
    if (role == 1) BAR_L();
  }
  asm volatile("" : "+s"(role));
  u32x4 kr[7], vr[4];
  if (role == 2) {
    prep_kv_load(b - 192, bk, bv, shk, kr, vr);
  }
  if (wid >= 8) {
    __builtin_amdgcn_s_setprio(3);
    const int a = wid - 8, fg = fq * 4 + a;
    if (b >= 192) {
      BAR_L();
      prep_w_load(b - 192, a, wo, wqkv);
      conv_stage_h(b * 2, a, ssmk);
      BAR_L();
      WAIT_VM(8);
      prep_w_store(c, b - 192, a);
      BAR_L();
    }
    const unsigned dskip = *(const unsigned*)(ssmd + b * 2);
    if (b >= 192) set_flag_last(c, FL_1 + b * 128, CT_W, 3);
    if (!uearly) {
      wait_flags(c, (lane_id() & 7) * 1024, FL_U + (b >> 5) * 128, 1);
      conv_issue_u(c, b * 2, a);
    }
    conv_stage_front(b * 2, a, ssmk);
    asm volatile("s_waitcnt vmcnt(0) lgkmcnt(0)" ::: "memory");
    if (lane_id() == 0) lds_add(CT_CV, 1u);
    lds_wait(CT_CV, 4);
#pragma unroll 1
    for (int i = 0; i < 2; i++) {
      const float dc = i ? bhi(dskip) : blo(dskip);
      if ((a & 1) == 0) conv_compute<7>(c, b * 2 + i, dc, i * 4 + a, i ? CV_B1 : CV_H);
      else conv_compute<5>(c, b * 2 + i, dc, i * 4 + a, i ? CV_B1 : CV_H);
    }
    WAIT_LGKM();
    if (lane_id() == 0) lds_add(CT_CV, 1u);
    lds_wait(CT_CV, 8);
    kv_fetch(c, false, h, pfirst, a);
    const int lane = lane_id(), n16 = lane & 15, g = lane >> 4;
    static_assert(WS_X2F == WS_OF + 4 * MB, "one fill covers both");
#pragma unroll
    for (int i = 0; i < 6; i++) st16(c.W, lane * 16 + i * 1024, WS_OF + b * 24576 + a * 6144, u32x4{BLANK, BLANK, BLANK, BLANK});
    __builtin_amdgcn_raw_buffer_store_b32(BLANK, c.W, lane * 4, WS_X2S + b * 1024 + a * 256, 16);
    wait_flags(c, lane * 128, FL_1 + 192 * 128, 1);
    set_flag_last(c, FL_Y + b * 128, CT_Y, 3);
    u32x4 wf[NWR], xr[2];
    L<u32x4>(ATT_ONES + a * 2048 + g * 512 + 256 + n16 * 16) = ld16(c.W, lane * 16, WS_WOP + fg * 32768 + 28 * 1024);
#pragma unroll
    for (int i = 0; i < NWR; i++) wf[i] = ld16(c.W, lane * 16 + (i & 3) * 1024, WS_WOP + fg * 32768 + (i >> 2) * 4096);
    dma<3, true>(c.Wd, lane * 16, WS_WOP + fg * 32768 + 29 * 1024, OP_W + a * 3072);
#pragma unroll
    for (int t = 0; t < 2; t++) xr[t] = *(const u32x4*)(x + (size_t)(rg * 32 + t * 16 + n16) * D + fg * 32 + g * 8);
    {
      const unsigned o = a == 0   ? FL_O + (rg * 32 + (lane & 31)) * 128 + (lane >> 5) * (2048 * 128)
                         : a == 1 ? WS_GRAN + rg * 2048 + (lane & 15) * 128 + ((lane >> 4) & 1) * (64 * 2048)
                                  : FL_K + (h * 8 + (lane & 7)) * 128;
      const u32x2 t = ld8(c.W, o, 0);
      asm volatile("" ::"v"(t));
    }
    wait_flags(c, (lane & 3) * 512, FL_1 + (h >> 3) * 128, 1);
    wait_flags(c, lane * 128, FL_Y + a * 8192, 1);
    if (lane == 0) lds_add(CT_PG, 1u);
    lds_wait(CT_PG, 4);
    const unsigned yv = (lane >> 2) * (S * 2) + (lane & 3) * 16;
    dma<1, true>(c.Wd, yv, WS_YT + fg * 32 * (S * 2) + rg * 64, OP_Y + a * 2048);
    dma<1, true>(c.Wd, yv, WS_YT + (fg * 32 + 16) * (S * 2) + rg * 64, OP_Y + a * 2048 + 1024);
    WAIT_VM(0);
    f32x4 res[2][2];
#pragma unroll
    for (int t = 0; t < 2; t++)
#pragma unroll
      for (int i = 0; i < 2; i++) {
        res[t][i] = f32x4{blo(xr[t][2 * i]), bhi(xr[t][2 * i]), blo(xr[t][2 * i + 1]), bhi(xr[t][2 * i + 1])};
        PIN(res[t][i]);
      }
    oproj_phase(c, 0, wf, res, out, rg, fq);
    BAR_L();
    kv_fetch(c, true, h, panel, a);
    oproj_phase(c, 1, wf, res, out, rg, fq);
    if (b == 0 && a == 0 && lane_id() == 0) st8(c.W, 0, WS_EPOCH, u32x2{(unsigned)epoch, (unsigned)(epoch >> 32)});
    return;
  }
  {
    const unsigned ONE2 = (tid & 15) == 0 ? 0x3f803f80u : 0u;
    L<u32x4>(ATT_ONES + tid * 16) = u32x4{ONE2, ONE2, ONE2, ONE2};
  }
  if (b < 192) {
    read_epoch();
    if (ct >= 8) gemm1<true>(c, rt, ct);
    else gemm1<false>(c, rt, ct);
    set_flag_last(c, FL_1 + (rt * 12 + ct) * 128, CT_1, 7);
  } else {
    prep_kv(c, b - 192, kr, vr);
    read_epoch();
    set_flag_last(c, FL_U + (b - 192) * 128, CT_1, 7);
    BAR_L();
  }
  int layer = 0;
  asm volatile("" : "+s"(layer));
#pragma unroll 1
  for (;; layer++) {
    u32x4 qv;
    if (layer == 0) {
      const int lane = lane_id();
      const unsigned go = CT_GO + (wid >> 2) * 4, ks = WS_KVF + h * 131072 + wid * 1024;
      const int k = lane & 7;
      const unsigned fo = (k < 6 ? ((pfirst + (k >> 1)) & 7) * 24 + 4 + (k & 1) * 4 : panel * 24) * 128;
      const unsigned fb = FL_1 + ((wid >> 2) * 12 + (h >> 3)) * 128;
      unsigned seen = 0;
      if ((wid & 3) == 0) {
#pragma unroll 1
        for (unsigned spins = 0; spins < SPIN_CAP; spins++) {
          seen = (unsigned)poll_flags(c, fo, fb) & 0xffu;
          if ((seen & 0xc3u) == 0xc3u) break;
          if (b >= 192) __builtin_amdgcn_s_sleep(8);
        }
        if (lane == 0) LV<unsigned>(go) = seen == 0xffu ? 2u : 1u;
      } else {
        lds_wait(go, 1);
      }
      asm volatile("s_nop 4\n\tbuffer_load_dwordx4 %0, %1, %2, %3 offen sc1"
                   : "=v"(qv) : "v"(lane * 16), "s"(c.Wd), "s"(WS_QF + (h * 64 + panel * 8 + wid) * 1024) : "memory");
      dma<1, true>(c.Wd, lane * 16, ks + pfirst * 16384, wid * 1024);
      dma<1, true>(c.Wd, lane * 16, ks + pfirst * 16384 + 8192, 8192 + wid * 1024);
      asm volatile("s_waitcnt vmcnt(0)" : "+v"(qv)::"memory");
      if (lane == 0) lds_add(KV_CNT, 1u);
      if ((wid & 3) == 0 && seen != 0xffu) {
        wait_flags(c, fo, fb, 0);
        if (lane == 0) LV<unsigned>(go) = 2u;
      }
      lds_wait(go, 2);
#pragma unroll
      for (int i = 2; i < 6; i++)
        dma<1, true>(c.Wd, lane * 16, ks + ((pfirst + (i >> 1)) & 7) * 16384 + (i & 1) * 8192, (i >> 1) * 16384 + (i & 1) * 8192 + wid * 1024);
      lds_wait(KV_CNT, 8);
    } else {
      qv = qkv2_phase(c, h, panel);
      BAR_L();
    }
    const unsigned hs = FL_O + ((layer * 64 + rg) * 32 + wid * 4) * 128;
    const u32x2 hw = attn_main(c, qv, layer, h, panel, hs);
    const int lane = lane_id();
    const unsigned os = WS_OF + (layer * 64 + rg) * 32768 + wid * 2048;
    u32x4 ot[4];
    if (!all(hw[0] == c.tag[0] && hw[1] == c.tag[1])) wait_flags(c, (lane & 3) * 128, hs, 0);
    poll4(c, ot, lane * 16, os, 16384);
    lds_wait(CT_AT, 8 + 8 * layer);
#pragma unroll
    for (int i = 0; i < 4; i++) L<u32x4>(MG_O + (i >> 1) * 16384 + wid * 2048 + (i & 1) * 1024 + lane * 16) = ot[i];
    BAR_L();
    if (layer == 1) break;
    dma<3, true>(c.Wd, lane * 16, WS_WQP + (h * 48 + wid * 6) * 1024, MG_W + wid * 6144);
    dma<3, true>(c.Wd, lane * 16, WS_WQP + (h * 48 + wid * 6 + 3) * 1024, MG_W + wid * 6144 + 3072);
    BAR_L();
    WAIT_BAR(0);
  }
}

#if defined(__HIP_DEVICE_COMPILE__)
#pragma clang attribute pop
#endif

extern "C" void kernel_launch(void* const* d_in, const int* in_sizes, int n_in, void* d_out, int out_size, void* d_ws,
                              size_t ws_size, hipStream_t stream) {
  (void)hipFuncSetAttribute((const void*)k_mega, hipFuncAttributeMaxDynamicSharedMemorySize, ATT_LDS);
  k_mega<<<256, 768, ATT_LDS, stream>>>((const u16*)d_in[0], (const u16*)d_in[1], (const u16*)d_in[2], (const u16*)d_in[4],
                                           (const u16*)d_in[5], (const u16*)d_in[6], (const u16*)d_in[7], (const u16*)d_in[8],
                                           (char*)d_ws, (u16*)d_out);
}
```
